```python
import torch

def _softmax(x, axis=-1):
    return torch.softmax(x, dim=axis)
def _gelu(x):
    return torch.nn.functional.gelu(x, approximate="tanh")
def _silu(x):
    return torch.nn.functional.silu(x)
def _sigmoid(x):
    return torch.sigmoid(x)
def _layernorm(x, axis=-1, eps=1e-5):
    m = x.mean(dim=axis, keepdim=True)
    v = x.var(dim=axis, keepdim=True, unbiased=False)
    return (x - m) / torch.sqrt(v + eps)
def _scatter_add(tgt, idx, src, axis=0):
    return tgt.clone().index_add_(axis, idx, src)
def _segment_sum(src, seg, n):
    out = torch.zeros((n,) + src.shape[1:], dtype=src.dtype)
    return out.index_add_(0, seg, src)
def _pad(x, pw):
    flat = tuple(v for pair in reversed(pw) for v in pair)
    return torch.nn.functional.pad(x, flat)

kernel_name = 'gather_scatter__cheap_equivariant_egnn__graph'

def setup_inputs():

    _w = (lambda a: (lambda t: t.to(torch.bfloat16) if t.is_floating_point() else t)(torch.as_tensor(a)))
    N, deg, D, De = 10000, 16, 256, max(16, 1)
    E = N * deg
    return {
        "h": _w((lambda *s: torch.randn(*s, dtype=torch.bfloat16) * 0.1)(N, D)),
        "pos": _w((lambda *s: torch.randn(*s, dtype=torch.bfloat16) * 0.8)(N, 3)),
        "e_attr": _w((lambda *s: torch.randn(*s, dtype=torch.bfloat16) * 0.8)(E, De)),
        "src": _w((lambda lo, hi, *s: torch.randint(lo, hi, s))(0, N, E).to(torch.int64)),
        "dst": _w((lambda lo, hi, *s: torch.randint(lo, hi, s))(0, N, E).to(torch.int64)),
        "w_e": _w(0.1 * (lambda *s: torch.randn(*s, dtype=torch.bfloat16) * 0.8)(2 * D + 1 + De, D)),
        "w_x": _w(0.1 * (lambda *s: torch.randn(*s, dtype=torch.bfloat16) * 0.1)(D, 1)),
        "w_h": _w(0.1 * (lambda *s: torch.randn(*s, dtype=torch.bfloat16) * 0.1)(2 * D, D)),
    }

def reference(h, pos, e_attr, src, dst, w_e, w_x, w_h):
    N, D = h.shape[0], h.shape[1]
    diff = pos[dst] - pos[src]
    dist2 = torch.sum(diff * diff, axis=-1, keepdims=True)
    e_in = torch.concatenate([h[dst], h[src], dist2, e_attr], axis=-1)
    m_ij = _silu(e_in @ w_e)
    coord_scale = m_ij @ w_x
    pos_new = pos + _segment_sum(diff * coord_scale, dst, N)
    m_i = _segment_sum(m_ij, dst, N)
    h_new = h + _silu(torch.concatenate([h, m_i], axis=-1) @ w_h)
    return h_new, pos_new
```

```cpp
#include <hip/hip_runtime.h>
#include <hip/hip_bf16.h>
typedef unsigned short u16;
static __device__ __forceinline__ float bf2f(u16 x) { return __uint_as_float(((unsigned)x) << 16); }
static __device__ __forceinline__ u16 f2bf(float f) { unsigned u = __float_as_uint(f); u += 0x7fff + ((u >> 16) & 1); return (u16)(u >> 16); }
constexpr int N = 10000, E = 160000, D = 256;
__global__ void k_ab(const u16* h, const u16* we, float* AB, float* mi, float* pd) {
  int n = blockIdx.x, f = threadIdx.x;
  __shared__ float hs[256];
  hs[f] = bf2f(h[n * D + f]);
  __syncthreads();
  float a = 0, b = 0;
  for (int k = 0; k < 256; ++k) { a += hs[k] * bf2f(we[k * D + f]); b += hs[k] * bf2f(we[(256 + k) * D + f]); }
  AB[n * 512 + f] = a; AB[n * 512 + 256 + f] = b;
  mi[n * D + f] = 0; if (f < 4) pd[n * 4 + f] = 0;
}
__global__ void k_edge(const u16* pos, const u16* ea, const int* src, const int* dst, const u16* we, const u16* wx, const float* AB, float* mi, float* pd) {
  int e = blockIdx.x, f = threadIdx.x;
  int s = src[e], d = dst[e];
  float df[3]; float d2 = 0;
  for (int c = 0; c < 3; ++c) { df[c] = bf2f(f2bf(bf2f(pos[d * 3 + c]) - bf2f(pos[s * 3 + c]))); d2 += bf2f(f2bf(df[c] * df[c])); }
  d2 = bf2f(f2bf(d2));
  float p = AB[d * 512 + f] + AB[s * 512 + 256 + f] + d2 * bf2f(we[512 * D + f]);
  for (int k = 0; k < 16; ++k) p += bf2f(ea[e * 16 + k]) * bf2f(we[(513 + k) * D + f]);
  float m = p / (1.f + __expf(-p));
  atomicAdd(&mi[d * D + f], m);
  float cs = m * bf2f(wx[f]);
  for (int o = 32; o; o >>= 1) cs += __shfl_xor(cs, o);
  if ((f & 63) == 0) for (int c = 0; c < 3; ++c) atomicAdd(&pd[d * 4 + c], df[c] * cs);
}
__global__ void k_fin(const u16* h, const u16* pos, const u16* wh, const float* mi, const float* pd, u16* out) {
  int n = blockIdx.x, f = threadIdx.x;
  __shared__ float hs[512];
  hs[f] = bf2f(h[n * D + f]); hs[256 + f] = mi[n * D + f];
  __syncthreads();
  float a = 0;
  for (int k = 0; k < 512; ++k) a += hs[k] * bf2f(wh[k * D + f]);
  out[n * D + f] = f2bf(hs[f] + a / (1.f + __expf(-a)));
  if (f < 3) out[N * D + n * 3 + f] = f2bf(bf2f(pos[n * 3 + f]) + pd[n * 4 + f]);
}
extern "C" void kernel_launch(void* const* d_in, const int* in_sizes, int n_in, void* d_out, int out_size, void* d_ws, size_t ws_size, hipStream_t stream) {
  const u16* h = (const u16*)d_in[0]; const u16* pos = (const u16*)d_in[1]; const u16* ea = (const u16*)d_in[2];
  const int* src = (const int*)d_in[3]; const int* dst = (const int*)d_in[4];
  const u16* we = (const u16*)d_in[5]; const u16* wx = (const u16*)d_in[6]; const u16* wh = (const u16*)d_in[7];
  float* AB = (float*)d_ws; float* mi = AB + N * 512; float* pd = mi + N * D;
  k_ab<<<N, 256, 0, stream>>>(h, we, AB, mi, pd);
  k_edge<<<E, 256, 0, stream>>>(pos, ea, src, dst, we, wx, AB, mi, pd);
  k_fin<<<N, 256, 0, stream>>>(h, pos, wh, mi, pd, (u16*)d_out);
}
```

```cpp
#include <hip/hip_runtime.h>
#include <stdint.h>

typedef unsigned short u16;
typedef unsigned int u32;
typedef short bf16x8 __attribute__((ext_vector_type(8)));
typedef short s16x4 __attribute__((ext_vector_type(4)));
typedef float f32x16 __attribute__((ext_vector_type(16)));
typedef float f32x4 __attribute__((ext_vector_type(4)));
typedef float f32x2 __attribute__((ext_vector_type(2)));
typedef __bf16 bf2 __attribute__((ext_vector_type(2)));
typedef u32 u32x4 __attribute__((ext_vector_type(4)));
typedef u32 u32x2 __attribute__((ext_vector_type(2)));
typedef __attribute__((address_space(3))) s16x4* lds_s16x4_p;
typedef __attribute__((address_space(3))) char* lds_char_p;
#define DI __device__ __forceinline__
#define MFMA32(a, b, c) __builtin_amdgcn_mfma_f32_32x32x16_bf16((a), (b), (c), 0, 0, 0)
#define MFMA16(a, b, c) __builtin_amdgcn_mfma_f32_16x16x32_bf16((a), (b), (c), 0, 0, 0)
#define SB() __builtin_amdgcn_sched_barrier(0)

constexpr int N = 10000, E = 160000;
constexpr int NPB = 40, NB = 250;
constexpr int NCH = 64, CHE = 2500;
constexpr int TCAP = 26;
constexpr float SCL = -1.4426950408889634f;
constexpr float ISCL = -0.6931471805599453f;
constexpr u32 OOB = 0x40000000u;

constexpr size_t WS_B = 0;
constexpr size_t WS_CELL = 5120000;
constexpr size_t WS_W1F = WS_CELL + (size_t)NB * NCH * 128;
constexpr size_t WS_WHF = WS_W1F + 131072;
constexpr size_t WS_W4F = WS_WHF + 262144;
constexpr size_t WS_WDP = WS_W4F + 8192;
constexpr size_t WS_WXF = WS_WDP + 1024;
constexpr size_t WS_POS4 = WS_WXF + 512;

DI float bf2f(u32 x) { return __uint_as_float(x << 16); }
DI u32 pk2(float a, float b) { f32x2 v = {a, b}; return __builtin_bit_cast(u32, __builtin_convertvector(v, bf2)); }
DI u32 f2bf(float a) { return pk2(a, 0.f) & 0xffffu; }
DI u32 lds_addr(const void* p) { return (u32)(uintptr_t)(lds_char_p)p; }
DI __amdgpu_buffer_rsrc_t mk_rsrc(const void* p, u32 bytes) { return __builtin_amdgcn_make_buffer_rsrc((void*)p, (short)0, (int)bytes, 0x00020000); }
DI void dma16(u32 m0v, u32 voff, __amdgpu_buffer_rsrc_t r, u32 soff) {
  asm volatile("s_nop 4\n\ts_mov_b32 m0, %0\n\ts_nop 0\n\tbuffer_load_dwordx4 %1, %2, %3 offen lds" ::"s"(m0v), "v"(voff), "s"(r), "s"(soff) : "memory");
}
DI void dma16f(u32 m0v, u32 voff, __amdgpu_buffer_rsrc_t r, u32 soff) {
  asm volatile("s_mov_b32 m0, %0\n\ts_nop 0\n\tbuffer_load_dwordx4 %1, %2, %3 offen lds" ::"s"(m0v), "v"(voff), "s"(r), "s"(soff) : "memory");
}
#define BAR_LGKM() asm volatile("s_waitcnt lgkmcnt(0)\n\ts_barrier" ::: "memory")
#define BAR_ALL() asm volatile("s_waitcnt vmcnt(0) lgkmcnt(0)\n\ts_barrier" ::: "memory")
DI bf16x8 ld128(const char* smem, u32 off) { return *(const bf16x8*)(smem + off); }
DI bf16x8 tr2(const char* smem, u32 o0, u32 o1) {
  s16x4 a = __builtin_amdgcn_ds_read_tr16_b64_v4i16((lds_s16x4_p)(lds_char_p)(smem + o0));
  s16x4 b = __builtin_amdgcn_ds_read_tr16_b64_v4i16((lds_s16x4_p)(lds_char_p)(smem + o1));
  return __builtin_shufflevector(a, b, 0, 1, 2, 3, 4, 5, 6, 7);
}
DI bf16x8 asbf(u32x4 v) { return __builtin_bit_cast(bf16x8, v); }

__global__ __launch_bounds__(512) void k1(const u16* __restrict__ h, const u16* __restrict__ pos, const int* __restrict__ src,
                                          const int* __restrict__ dst, const u16* __restrict__ we, const u16* __restrict__ wx,
                                          const u16* __restrict__ wh, char* __restrict__ ws) {
  __shared__ __attribute__((aligned(1024))) char smem[131072];
  const int bid = blockIdx.x, tid = threadIdx.x, lane = tid & 63;
  const int w = __builtin_amdgcn_readfirstlane(tid >> 6);
  if (bid < 160) {
    const int tile = (bid >> 4) * 8 + (bid & 7), half = (bid >> 3) & 1;
    if (tile >= 79) return;
    const int node0 = tile * 128;
    const u32 sb = lds_addr(smem);
    const __amdgpu_buffer_rsrc_t rW = mk_rsrc(we + 256 * 256, 256 * 512), rH = mk_rsrc(h, N * 512);
    {
      const u32 rq = lane >> 4;
      const u32 vW = (8 * w + rq) * 512 + half * 256 + 16 * ((lane & 15) ^ (((rq & 1) << 1) | ((rq & 2) << 2)));
      const u32 hr = node0 + 16 * w + (lane >> 3);
      const u32 vH0 = hr * 512 + 16 * ((lane & 7) ^ rq), vH1 = (hr + 8) * 512 + 16 * ((lane & 7) ^ (4 + rq));
#pragma unroll
      for (int st = 0; st < 4; ++st) {
        dma16(sb + st * 16384 + w * 2048, vW, rW, st * 32768);
        dma16(sb + st * 16384 + w * 2048 + 1024, vW, rW, st * 32768 + 2048);
        dma16(sb + 65536 + st * 16384 + w * 2048, vH0, rH, st * 128);
        dma16(sb + 65536 + st * 16384 + w * 2048 + 1024, vH1, rH, st * 128);
      }
    }
    const int fq = w & 1, nt = w >> 1, hh = lane >> 5;
    u32 aw[2];
    {
      const u32 blk = (lane >> 4) & 1, q = (lane & 15) >> 2, p = lane & 3, u = 4 * blk + p;
      const u32 fw = ((q & 1) << 1) | ((q & 2) << 2);
#pragma unroll
      for (int m = 0; m < 2; ++m) {
        const u32 v = 8 * (u & 1) + 4 * m + (u >> 1), ch = 8 * fq + (v >> 1);
        aw[m] = 256 * (8 * hh + q) + 16 * (ch ^ fw) + 8 * (v & 1);
      }
    }
    const u32 hrow = 32 * nt + (lane & 31);
    const u32 bh = 65536 + hrow * 128 + 16 * (hh ^ ((hrow >> 1) & 7));
    f32x16 acc0 = {}, acc1 = {};
#pragma unroll
    for (int st = 0; st < 4; ++st) {
      if (st == 0) asm volatile("s_waitcnt vmcnt(12)\n\ts_barrier" ::: "memory");
      if (st == 1) asm volatile("s_waitcnt vmcnt(8)\n\ts_barrier" ::: "memory");
      if (st == 2) asm volatile("s_waitcnt vmcnt(4)\n\ts_barrier" ::: "memory");
      if (st == 3) asm volatile("s_waitcnt vmcnt(0)\n\ts_barrier" ::: "memory");
#pragma unroll
      for (int kk = 0; kk < 4; ++kk) {
        const int ks = 4 * st + kk;
        const bf16x8 a0 = tr2(smem, aw[0] + 4096 * ks, aw[0] + 4096 * ks + 1024);
        const bf16x8 a1 = tr2(smem, aw[1] + 4096 * ks, aw[1] + 4096 * ks + 1024);
        const bf16x8 b = ld128(smem, (bh ^ (32 * kk)) + 16384 * st);
        acc0 = MFMA32(a0, b, acc0);
        acc1 = MFMA32(a1, b, acc1);
      }
    }
    const int node = node0 + hrow;
    if (node < N) {
      u32x4* o = (u32x4*)(ws + WS_B + (size_t)node * 512 + half * 256 + fq * 128 + hh * 64);
      u32x4 v;
#pragma unroll
      for (int j = 0; j < 2; ++j) {
#pragma unroll
        for (int i = 0; i < 4; ++i) v[i] = pk2(acc0[8 * j + 2 * i] * SCL, acc0[8 * j + 2 * i + 1] * SCL);
        o[j] = v;
      }
#pragma unroll
      for (int j = 0; j < 2; ++j) {
#pragma unroll
        for (int i = 0; i < 4; ++i) v[i] = pk2(acc1[8 * j + 2 * i] * SCL, acc1[8 * j + 2 * i + 1] * SCL);
        o[2 + j] = v;
      }
    }
  } else if (bid < 224) {
    const int c = bid - 160;
    u32* cnt = (u32*)smem;
    u32* stage = (u32*)(smem + 1024);
    const int4* d4 = (const int4*)(dst + c * CHE);
    const int4* s4 = (const int4*)(src + c * CHE);
    const bool has2 = tid < CHE / 4 - 512;
    const int i2 = has2 ? 512 + tid : tid;
    const int4 dv0 = d4[tid], sv0 = s4[tid], dv1 = d4[i2], sv1 = s4[i2];
    if (tid < 256) cnt[tid] = 0;
    __syncthreads();
    auto put = [&](int d, int s, int el) {
      const u32 b = (u32)d / NPB, dl = d - b * NPB;
      const u32 r = atomicAdd(&cnt[b], 1u);
      if (r < 31) stage[b * 36 + 1 + r] = (dl << 26) | ((u32)s << 12) | (u32)el;
    };
    put(dv0.x, sv0.x, 4 * tid); put(dv0.y, sv0.y, 4 * tid + 1); put(dv0.z, sv0.z, 4 * tid + 2); put(dv0.w, sv0.w, 4 * tid + 3);
    if (has2) { put(dv1.x, sv1.x, 4 * i2); put(dv1.y, sv1.y, 4 * i2 + 1); put(dv1.z, sv1.z, 4 * i2 + 2); put(dv1.w, sv1.w, 4 * i2 + 3); }
    __syncthreads();
    u32x4* cells = (u32x4*)(ws + WS_CELL);
#pragma unroll
    for (int it = 0; it < 4; ++it) {
      const int i = tid + 512 * it;
      if (i < NB * 8) {
        const int b = i >> 3, pc = i & 7;
        u32x4 v = *(const u32x4*)&stage[b * 36 + 4 * pc];
        if (pc == 0) v[0] = cnt[b];
        cells[(b * NCH + c) * 8 + pc] = v;
      }
    }
  } else if (bid < 248) {
#pragma unroll
    for (int it = 0; it < 2; ++it) {
      const int x = (bid - 224) * 1024 + 512 * it + tid;
      const u16* p;
      if (x < 8192) {
        const int ct = x >> 10, s = (x >> 6) & 15;
        p = we + (16 * s + 8 * (lane >> 5)) * 256 + 32 * ct + (lane & 31);
      } else {
        const int y = x - 8192, mt = y >> 11, s = (y >> 6) & 31, r = lane & 31;
        p = wh + (16 * s + 8 * (lane >> 5)) * 256 + 32 * mt + 16 * ((r >> 2) & 1) + 4 * (r >> 3) + (r & 3);
      }
      u32x4 v;
#pragma unroll
      for (int j = 0; j < 4; ++j) v[j] = (u32)p[(2 * j) * 256] | ((u32)p[(2 * j + 1) * 256] << 16);
      *(u32x4*)(ws + WS_W1F + (size_t)x * 16) = v;
    }
  } else if (bid == 248) {
    {
      const u16* p = we + (513 + 8 * (lane >> 5)) * 256 + 32 * w + (lane & 31);
      u32x4 v;
#pragma unroll
      for (int j = 0; j < 4; ++j) v[j] = pk2(bf2f(p[(2 * j) * 256]) * SCL, bf2f(p[(2 * j + 1) * 256]) * SCL);
      *(u32x4*)(ws + WS_W4F + tid * 16) = v;
    }
    if (tid < 256) {
      const float v = bf2f(we[512 * 256 + tid]) * SCL;
      const u32 hi = f2bf(v);
      ((u32*)(ws + WS_WDP))[tid] = hi | (f2bf(v - bf2f(hi)) << 16);
    }
    if (tid < 32) {
      const int f0 = 32 * (tid >> 2) + 16 * ((tid >> 1) & 1) + 4 * (tid & 1);
      u32x4 v;
      v[0] = (u32)wx[f0] | ((u32)wx[f0 + 1] << 16); v[1] = (u32)wx[f0 + 2] | ((u32)wx[f0 + 3] << 16);
      v[2] = (u32)wx[f0 + 8] | ((u32)wx[f0 + 9] << 16); v[3] = (u32)wx[f0 + 10] | ((u32)wx[f0 + 11] << 16);
      *(u32x4*)(ws + WS_WXF + tid * 16) = v;
    }
  } else {
    const int n0 = (bid - 249) * 2500;
#pragma unroll
    for (int it = 0; it < 5; ++it) {
      const int i = tid + 512 * it;
      if (i < 2500) {
        const u16* p = pos + (n0 + i) * 3;
        u32x2 v = {(u32)p[0] | ((u32)p[1] << 16), (u32)p[2]};
        *(u32x2*)(ws + WS_POS4 + (size_t)(n0 + i) * 8) = v;
      }
    }
  }
}

constexpr u32 LT = 2112;
constexpr u32 O_L = 0;
constexpr u32 O_A1 = O_L + TCAP * LT;
constexpr u32 O_SRC = O_A1 + TCAP * 1024;
constexpr u32 O_EL = O_SRC + TCAP * 128;
constexpr u32 O_G = 88320;
constexpr u32 O_H = O_G + 32768;
constexpr u32 O_M = O_H + 20480;
constexpr u32 O_CNT = O_M + 20480;
constexpr u32 O_POS = O_CNT + 64;
constexpr u32 O_WX = O_POS + 320;
constexpr u32 O_Z = O_WX + 512;
constexpr u32 LDS2 = O_Z + 512;

struct Frag { bf16x8 g0, g1, g2, g3, a1, a2; };

template <int NBLK_STATIC>
DI void act(const f32x16& x, u32x4& p0, u32x4& p1, int nblk) {
#pragma unroll
  for (int b = 0; b < 4; ++b) {
    u32 r0 = 0, r1 = 0;
    if (b < nblk) {
      f32x2 y0 = {x[4 * b], x[4 * b + 1]}, y1 = {x[4 * b + 2], x[4 * b + 3]}, e0, e1;
      e0[0] = __builtin_amdgcn_exp2f(y0[0]); e0[1] = __builtin_amdgcn_exp2f(y0[1]);
      e1[0] = __builtin_amdgcn_exp2f(y1[0]); e1[1] = __builtin_amdgcn_exp2f(y1[1]);
      e0 = e0 + 1.0f; e1 = e1 + 1.0f;
      e0[0] = __builtin_amdgcn_rcpf(e0[0]); e0[1] = __builtin_amdgcn_rcpf(e0[1]);
      e1[0] = __builtin_amdgcn_rcpf(e1[0]); e1[1] = __builtin_amdgcn_rcpf(e1[1]);
      e0 = e0 * y0; e1 = e1 * y1;
      r0 = pk2(e0[0], e0[1]); r1 = pk2(e1[0], e1[1]);
    }
    if (b < 2) { p0[2 * b] = r0; p0[2 * b + 1] = r1; } else { p1[2 * b - 4] = r0; p1[2 * b - 3] = r1; }
  }
}

template <class M1, class M2, class M3, class M4, class M5, class M6, class MID, class LATE>
DI void stage(const f32x16& x, u32x4& p0, u32x4& p1, M1 m1, M2 m2, M3 m3, M4 m4, M5 m5, M6 m6, MID mid, LATE late) {
  f32x2 e[8];
#define EX(i) e[i][0] = __builtin_amdgcn_exp2f(x[2 * (i)]); e[i][1] = __builtin_amdgcn_exp2f(x[2 * (i) + 1]);
#define RC(i) e[i][0] = __builtin_amdgcn_rcpf(e[i][0]); e[i][1] = __builtin_amdgcn_rcpf(e[i][1]);
  SB(); m1(); SB(); EX(0) EX(1) SB(); m2(); SB(); EX(2) EX(3) SB(); m3(); SB(); EX(4) EX(5) SB(); m4(); SB(); EX(6) EX(7) SB();
#pragma unroll
  for (int i = 0; i < 8; ++i) e[i] = e[i] + 1.0f;
  SB(); mid(); SB();
  RC(0) SB(); m5(); SB(); RC(1) RC(2) SB(); m6(); SB(); RC(3) SB(); late(); SB(); RC(4) RC(5) RC(6) RC(7) SB();
#pragma unroll
  for (int i = 0; i < 8; ++i) { f32x2 y = {x[2 * i], x[2 * i + 1]}; e[i] = e[i] * y; }
  SB();
#pragma unroll
  for (int i = 0; i < 4; ++i) { p0[i] = pk2(e[i][0], e[i][1]); p1[i] = pk2(e[4 + i][0], e[4 + i][1]); }
  SB();
#undef EX
#undef RC
}

__global__ __launch_bounds__(512) void k2(const u16* __restrict__ h, const u16* __restrict__ ea, const int* __restrict__ srcg,
                                          const int* __restrict__ dstg, const u16* __restrict__ we, const u16* __restrict__ wxg,
                                          const char* __restrict__ ws, u16* __restrict__ out) {
  __shared__ __attribute__((aligned(1024))) char smem[LDS2];
  const int tid = threadIdx.x, lane = tid & 63, hh = lane >> 5, r31 = lane & 31;
  const int w = __builtin_amdgcn_readfirstlane(tid >> 6), q = w & 3, p = w >> 2;
  const int b = blockIdx.x, node0 = b * NPB;
  const u32 sb = lds_addr(smem);
  const __amdgpu_buffer_rsrc_t rH = mk_rsrc(h, N * 512), rB = mk_rsrc(ws + WS_B, N * 512), rE = mk_rsrc(ea, E * 32);

  {
    const u32 row = 2 * w + hh;
    const u32 vh = (node0 + row) * 512 + 16 * (r31 ^ (row & 15));
    dma16(sb + O_H + w * 1024, vh, rH, 0);
    dma16(sb + O_H + w * 1024 + 8192, vh, rH, 8192);
    if (w < 4) dma16(sb + O_H + w * 1024 + 16384, vh, rH, 16384);
  }
  const u32x4 cw = *(const u32x4*)(ws + WS_CELL + (size_t)b * (NCH * 128) + tid * 16);
  bf16x8 w1[16];
  {
    const bf16x8* wp = (const bf16x8*)(ws + WS_W1F) + (2 * q + p) * 1024 + lane;
#pragma unroll
    for (int s = 0; s < 16; ++s) w1[s] = wp[s * 64];
  }
  const bf16x8 W40 = *(const bf16x8*)(ws + WS_W4F + (2 * q) * 1024 + lane * 16), W41 = *(const bf16x8*)(ws + WS_W4F + (2 * q + 1) * 1024 + lane * 16);
  const u32 wd0 = ((const u32*)(ws + WS_WDP))[64 * q + r31], wd1 = ((const u32*)(ws + WS_WDP))[64 * q + 32 + r31];
  u32x4 small = {0, 0, 0, 0};
  if (tid < 32) small = *(const u32x4*)(ws + WS_WXF + tid * 16);
  else if (tid < 52) small = *(const u32x4*)(ws + WS_POS4 + (size_t)node0 * 8 + (tid - 32) * 16);
  SB();
  {
    const u32x4 z = {0, 0, 0, 0}, oo = {OOB, OOB, OOB, OOB}, ff = {~0u, ~0u, ~0u, ~0u};
#pragma unroll
    for (int i = 0; i < 7; ++i) { const u32 o = (tid + 512 * i) * 16; if (o < O_A1) *(u32x4*)(smem + o) = z; }
    if (tid < TCAP * 8) *(u32x4*)(smem + O_SRC + tid * 16) = oo;
    else if (tid < TCAP * 16) *(u32x4*)(smem + O_EL + (tid - TCAP * 8) * 16) = ff;
    else if (tid < TCAP * 16 + 4) *(u32x4*)(smem + O_CNT + (tid - TCAP * 16) * 16) = z;
    else if (tid < TCAP * 16 + 36) *(u32x4*)(smem + O_Z + (tid - TCAP * 16 - 4) * 16) = z;
  }
  BAR_LGKM();
  u32* gcnt = (u32*)(smem + O_CNT);
  const u32 ccnt = (u32)__builtin_amdgcn_ds_swizzle((int)cw[0], 0x18);
  u32 rank[4];
  const u32 i8 = (tid & 7) * 4;
#pragma unroll
  for (int k = 0; k < 4; ++k) {
    rank[k] = ~0u;
    if (i8 + k >= 1 && i8 + k <= ccnt) rank[k] = atomicAdd(&gcnt[cw[k] >> 29], 1u);
  }
  if (ccnt > 31) gcnt[7] = 1;
  if (tid < 32) *(u32x4*)(smem + O_WX + tid * 16) = small;
  else if (tid < 52) *(u32x4*)(smem + O_POS + (tid - 32) * 16) = small;
  BAR_LGKM();
  int ng[5], tb[6];
  bool slow;
  {
    const u32x4 c0 = *(const u32x4*)(smem + O_CNT), c1 = *(const u32x4*)(smem + O_CNT + 16);
    ng[0] = __builtin_amdgcn_readfirstlane(c0[0]); ng[1] = __builtin_amdgcn_readfirstlane(c0[1]); ng[2] = __builtin_amdgcn_readfirstlane(c0[2]);
    ng[3] = __builtin_amdgcn_readfirstlane(c0[3]); ng[4] = __builtin_amdgcn_readfirstlane(c1[0]);
    tb[0] = 0;
#pragma unroll
    for (int g = 0; g < 5; ++g) tb[g + 1] = tb[g] + ((ng[g] + 31) >> 5);
    slow = __builtin_amdgcn_readfirstlane(c1[3]) != 0 || tb[5] > TCAP;
  }
  float* pdp = (float*)(smem + O_EL);
  if (!slow) {
    {
      const u32 tbp = (u32)tb[1] << 6 | (u32)tb[2] << 12 | (u32)tb[3] << 18 | (u32)tb[4] << 24;
      const u32 ebase = (tid >> 3) * CHE;
#pragma unroll
      for (int k = 0; k < 4; ++k)
        if (rank[k] != ~0u) {
          const u32 x = cw[k], slot = (((tbp >> (6 * (x >> 29))) & 63) << 5) + rank[k];
          *(u32*)(smem + O_EL + slot * 4) = (ebase + (x & 4095)) | ((x >> 26) << 18);
          *(u32*)(smem + O_SRC + (slot >> 5) * 128 + (slot & 7) * 16 + ((slot >> 3) & 3) * 4) = ((x >> 12) & 0x3fff) << 9;
        }
    }
    BAR_ALL();
    const u32 gsw0 = 16 * ((lane & 7) ^ (lane >> 4)), gsw1 = gsw0 ^ 64;
    const u32 gm0 = sb + O_G + w * 4096, gso = q * 128;
    const u32 srd = O_SRC + (lane >> 3) * 16;
    auto gdma = [&](u32x4 s) {
      dma16f(gm0, s[0] + gsw0, rB, gso); dma16f(gm0 + 1024, s[1] + gsw1, rB, gso);
      dma16f(gm0 + 2048, s[2] + gsw0, rB, gso); dma16f(gm0 + 3072, s[3] + gsw1, rB, gso);
    };
    const int t0 = p ? tb[2] : 0, t1 = p ? tb[4] : tb[2], t2 = p ? tb[5] : tb[4], t3 = tb[5];
    auto nextt = [&](int t) { return t + 1 == t1 ? t2 : t + 1; };
    {
      const int tf = t0 < t1 ? t0 : t2;
      if (tf < t3) gdma(*(const u32x4*)(smem + srd + tf * 128));
    }
#pragma unroll
    for (int i = 0; i < 4; ++i) {
      const int t = w + 8 * i;
      if (t < t3) {
        const u32 en = *(const u32*)(smem + O_EL + (t * 32 + r31) * 4);
        dma16f(sb + O_A1 + t * 1024, en == ~0u ? OOB : (en & 0x3ffff) * 32 + 16 * hh, rE, 0);
      }
    }
    u32 en[2]; u32x2 ps[2];
#pragma unroll
    for (int i = 0; i < 2; ++i) {
      const u32 s = tid + 512 * i;
      en[i] = ~0u;
      if (s < TCAP * 32) {
        en[i] = *(const u32*)(smem + O_EL + s * 4);
        const u32 so = *(const u32*)(smem + O_SRC + (s >> 5) * 128 + (s & 7) * 16 + ((s >> 3) & 3) * 4);
        if (en[i] != ~0u) ps[i] = *(const u32x2*)(ws + WS_POS4 + (so >> 6));
      }
    }
    SB();
    u32x4 own0, own1;
    {
      const int blk = r31 >> 4, gh = (r31 >> 2) & 1, j = (r31 & 3) + 4 * ((r31 >> 3) & 1);
      const u32 na = blk == 0 ? 8 * gh + j : 32 + j, nb2 = 16 + 8 * gh + j;
      const u32 aa = O_H + na * 512 + 16 * (hh ^ (na & 15)), ab = O_H + nb2 * 512 + 16 * (hh ^ (nb2 & 15));
      f32x16 ca = {}, cb = {};
#pragma unroll
      for (int s = 0; s < 16; ++s) {
        ca = MFMA32(ld128(smem, (aa ^ (32 * (s & 7))) + 256 * (s >> 3)), w1[s], ca);
        cb = MFMA32(ld128(smem, (ab ^ (32 * (s & 7))) + 256 * (s >> 3)), w1[s], cb);
      }
      u32x4 pa, pb, pc;
#pragma unroll
      for (int i = 0; i < 4; ++i) {
        pa[i] = pk2(ca[2 * i] * SCL, ca[2 * i + 1] * SCL);
        pb[i] = pk2(ca[8 + 2 * i] * SCL, ca[9 + 2 * i] * SCL);
        pc[i] = pk2(cb[2 * i] * SCL, cb[2 * i + 1] * SCL);
      }
      u32x4* ex = (u32x4*)(smem + O_M + q * 3072) + lane;
      if (p == 0) { ex[0] = pc; own0 = pa; own1 = pb; }
      else { ex[64] = pa; ex[128] = pb; own0 = pc; own1 = pc; }
    }
    SB();
#pragma unroll
    for (int i = 0; i < 2; ++i)
      if (en[i] != ~0u) {
        const u32 s = tid + 512 * i, dl = en[i] >> 18;
        const u32x2 pd = *(const u32x2*)(smem + O_POS + dl * 8);
        const u32 dxy = pk2(bf2f(pd[0] & 0xffff) - bf2f(ps[i][0] & 0xffff), bf2f(pd[0] >> 16) - bf2f(ps[i][0] >> 16));
        const u32 dz = f2bf(bf2f(pd[1] & 0xffff) - bf2f(ps[i][1] & 0xffff));
        const float fx = bf2f(dxy & 0xffff), fy = bf2f(dxy >> 16), fz = bf2f(dz);
        const u32 sxy = pk2(fx * fx, fy * fy), sz = f2bf(fz * fz);
        const u32 d2 = f2bf(bf2f(sxy & 0xffff) + bf2f(sxy >> 16) + bf2f(sz));
        u16* lp = (u16*)(smem + O_L + (s >> 5) * LT + ((s >> 4) & 1) * 1056 + ((s >> 2) & 1) * 528 + 2 * ((s & 3) + 4 * ((s >> 3) & 1)));
        const u32 n = dl & 7;
        lp[n * 8] = 0x3f80; lp[64 + n * 8] = (u16)dxy; lp[128 + n * 8] = (u16)(dxy >> 16); lp[192 + n * 8] = (u16)dz; lp[256] = (u16)d2;
      }
    BAR_ALL();
    u32x4 PA0, PA1, PB0, PB1;
    {
      const u32x4* ex = (const u32x4*)(smem + O_M + q * 3072) + lane;
      if (p == 0) { PA0 = own0; PB0 = own1; PA1 = ex[64]; PB1 = ex[128]; }
      else { PA0 = ex[0]; PA1 = own0; PB0 = PA0; PB1 = PA1; }
    }
    BAR_LGKM();
    bf16x8 I0, I1;
    {
      u32x4 a, c;
#pragma unroll
      for (int i = 0; i < 4; ++i) {
        const int k = 8 * hh + 2 * i;
        a[i] = (r31 == k ? 0x3f80u : 0u) | (r31 == k + 1 ? 0x3f800000u : 0u);
        c[i] = (r31 == k + 16 ? 0x3f80u : 0u) | (r31 == k + 17 ? 0x3f800000u : 0u);
      }
      I0 = asbf(a); I1 = asbf(c);
    }
    const u32 ga = O_G + w * 4096 + r31 * 128 + 16 * (hh ^ ((r31 >> 1) & 7));
    const u32 la = O_L + hh * 528 + r31 * 16, a1a = O_A1 + lane * 16;
    const u32 tra = O_L + ((lane >> 4) & 1) * 1056 + (lane & 1) * 528 + 8 * ((lane >> 1) & 1);
    const int ngr = p ? 2 : 3;
    for (int gi = 0; gi < ngr; ++gi) {
      const int g = gi == 2 ? 4 : 2 * p + gi;
      const int n = g == 0 ? ng[0] : g == 1 ? ng[1] : g == 2 ? ng[2] : g == 3 ? ng[3] : ng[4];
      const int ts = g == 0 ? tb[0] : g == 1 ? tb[1] : g == 2 ? tb[2] : g == 3 ? tb[3] : tb[4];
      const int te = ts + ((n + 31) >> 5);
      const bool oh = hh == (gi & 1);
      bf16x8 B20, B21;
      {
        const u32x4 s0 = gi == 2 ? PB0 : PA0, s1 = gi == 2 ? PB1 : PA1;
        u32x4 a = {oh ? s0[0] : wd0, oh ? s0[1] : 0u, oh ? s0[2] : 0u, oh ? s0[3] : 0u};
        u32x4 c = {oh ? s1[0] : wd1, oh ? s1[1] : 0u, oh ? s1[2] : 0u, oh ? s1[3] : 0u};
        B20 = asbf(a); B21 = asbf(c);
      }
      const u32 tr0 = tra + (oh ? ((lane >> 2) & 3) * 16 : 512), tr1 = tra + (oh ? ((lane >> 2) & 3) * 16 + 64 : 512);
      f32x16 Y0 = {}, Y1 = {};
      if (n > 0) {
        Frag f; u32x4 ns;
        auto rdfrag = [&](int t) {
          asm volatile("s_waitcnt vmcnt(0)" ::: "memory");
          f.g0 = ld128(smem, ga); f.g1 = ld128(smem, ga ^ 32); f.a1 = ld128(smem, a1a + t * 1024); f.a2 = tr2(smem, tr0 + t * LT, tr1 + t * LT);
          f.g2 = ld128(smem, ga ^ 64); f.g3 = ld128(smem, ga ^ 96);
          const int tn = nextt(t);
          ns = *(const u32x4*)(smem + srd + (tn < t3 ? tn : t) * 128);
        };
        auto issue = [&](int t) {
          asm volatile("s_waitcnt lgkmcnt(0)" ::: "memory");
          if (nextt(t) < t3) gdma(ns);
        };
        f32x16 X0, X1; u32x4 P00, P01, P10 = {0, 0, 0, 0}, P11 = {0, 0, 0, 0};
        bf16x8 L0, L1;
        const f32x16 zz = {};
        rdfrag(ts); issue(ts);
        L0 = ld128(smem, la + ts * LT); L1 = L0;
        X0 = MFMA32(f.a1, W40, zz); X0 = MFMA32(f.a2, B20, X0); X0 = MFMA32(f.g0, I0, X0); X0 = MFMA32(f.g1, I1, X0);
        for (int t = ts; t < te - 1; ++t) {
          stage(X0, P00, P01,
                [&] { X1 = MFMA32(f.a1, W41, zz); }, [&] { X1 = MFMA32(f.a2, B21, X1); }, [&] { X1 = MFMA32(f.g2, I0, X1); }, [&] { X1 = MFMA32(f.g3, I1, X1); },
                [&] { Y1 = MFMA32(asbf(P10), L0, Y1); }, [&] { Y1 = MFMA32(asbf(P11), L1, Y1); },
                [&] { rdfrag(t + 1); },
                [&] { L0 = ld128(smem, la + t * LT); L1 = ld128(smem, la + t * LT + 1056); });
          issue(t + 1);
          stage(X1, P10, P11,
                [&] { X0 = MFMA32(f.a1, W40, zz); }, [&] { X0 = MFMA32(f.a2, B20, X0); }, [&] { X0 = MFMA32(f.g0, I0, X0); }, [&] { X0 = MFMA32(f.g1, I1, X0); },
                [&] { Y0 = MFMA32(asbf(P00), L0, Y0); }, [&] { Y0 = MFMA32(asbf(P01), L1, Y0); },
                [&] {}, [&] {});
        }
        {
          const int t = te - 1, nblk = (n - 32 * (t - ts) + 7) >> 3;
          X1 = MFMA32(f.a1, W41, zz); Y1 = MFMA32(asbf(P10), L0, Y1); X1 = MFMA32(f.a2, B21, X1); Y1 = MFMA32(asbf(P11), L1, Y1);
          X1 = MFMA32(f.g2, I0, X1); X1 = MFMA32(f.g3, I1, X1);
          L0 = ld128(smem, la + t * LT); L1 = ld128(smem, la + t * LT + 1056);
          act<0>(X0, P00, P01, nblk);
          Y0 = MFMA32(asbf(P00), L0, Y0);
          if (nblk > 2) Y0 = MFMA32(asbf(P01), L1, Y0);
          act<0>(X1, P10, P11, nblk);
          Y1 = MFMA32(asbf(P10), L0, Y1);
          if (nblk > 2) Y1 = MFMA32(asbf(P11), L1, Y1);
        }
      }
      {
        u32x4 k00, k01, k10, k11;
#pragma unroll
        for (int i = 0; i < 4; ++i) {
          k00[i] = pk2(Y0[2 * i] * ISCL, Y0[2 * i + 1] * ISCL); k01[i] = pk2(Y0[8 + 2 * i] * ISCL, Y0[9 + 2 * i] * ISCL);
          k10[i] = pk2(Y1[2 * i] * ISCL, Y1[2 * i + 1] * ISCL); k11[i] = pk2(Y1[8 + 2 * i] * ISCL, Y1[9 + 2 * i] * ISCL);
        }
        const u32 wxa = O_WX + q * 128 + hh * 16;
        f32x16 Z = {};
        Z = MFMA32(ld128(smem, wxa), asbf(k00), Z); Z = MFMA32(ld128(smem, wxa + 32), asbf(k01), Z);
        Z = MFMA32(ld128(smem, wxa + 64), asbf(k10), Z); Z = MFMA32(ld128(smem, wxa + 96), asbf(k11), Z);
        if (r31 < 8) {
          const u32 nd = 8 * g + r31;
          const u32 ma = O_M + nd * 512 + 8 * hh + (q >> 1) * 256 + 16 * (((q & 1) * 8) ^ (nd & 15));
          u32x2 v;
          v = u32x2{k00[0], k00[1]}; *(u32x2*)(smem + ma) = v;         v = u32x2{k00[2], k00[3]}; *(u32x2*)(smem + (ma ^ 16)) = v;
          v = u32x2{k01[0], k01[1]}; *(u32x2*)(smem + (ma ^ 32)) = v;  v = u32x2{k01[2], k01[3]}; *(u32x2*)(smem + (ma ^ 48)) = v;
          v = u32x2{k10[0], k10[1]}; *(u32x2*)(smem + (ma ^ 64)) = v;  v = u32x2{k10[2], k10[3]}; *(u32x2*)(smem + (ma ^ 80)) = v;
          v = u32x2{k11[0], k11[1]}; *(u32x2*)(smem + (ma ^ 96)) = v;  v = u32x2{k11[2], k11[3]}; *(u32x2*)(smem + (ma ^ 112)) = v;
        }
        if (lane < 32) pdp[q * 160 + g * 32 + lane] = Z[0];
      }
    }
  } else {
    float* Af = (float*)smem;
    float* Mf = (float*)(smem + 40960);
    float* pf = (float*)(smem + 81920);
    u32* lst = (u32*)(smem + O_G);
    BAR_ALL();
    for (int i = tid; i < NPB * 256; i += 512) {
      const int n = i >> 8, f = i & 255;
      float a = 0.f;
      for (int k = 0; k < 256; ++k) a += bf2f(h[(node0 + n) * 256 + k]) * bf2f(we[k * 256 + f]);
      Af[i] = a * SCL; Mf[i] = 0.f;
    }
    if (tid < 160) pf[tid] = 0.f;
    for (int e0 = 0; e0 < E; e0 += 512) {
      if (tid == 0) gcnt[6] = 0;
      __syncthreads();
      const int e = e0 + tid;
      if (e < E) { const u32 dl = (u32)(dstg[e] - node0); if (dl < (u32)NPB) lst[atomicAdd(&gcnt[6], 1u)] = e; }
      __syncthreads();
      const int cnt = gcnt[6];
      for (int i = w; i < cnt; i += 8) {
        const int ee = lst[i], s = srcg[ee], dl = dstg[ee] - node0;
        const u32x2 pd = *(const u32x2*)(smem + O_POS + dl * 8), psv = *(const u32x2*)(ws + WS_POS4 + (size_t)s * 8);
        const float fx = bf2f(f2bf(bf2f(pd[0] & 0xffff) - bf2f(psv[0] & 0xffff))), fy = bf2f(f2bf(bf2f(pd[0] >> 16) - bf2f(psv[0] >> 16)));
        const float fz = bf2f(f2bf(bf2f(pd[1] & 0xffff) - bf2f(psv[1] & 0xffff)));
        const float d2 = bf2f(f2bf(bf2f(f2bf(fx * fx)) + bf2f(f2bf(fy * fy)) + bf2f(f2bf(fz * fz))));
        float cs = 0.f;
        for (int c = 0; c < 4; ++c) {
          const int f = lane + 64 * c;
          float y = d2 * bf2f(we[512 * 256 + f]);
          for (int k = 0; k < 16; ++k) y += bf2f(ea[ee * 16 + k]) * bf2f(we[(513 + k) * 256 + f]);
          y = y * SCL + Af[dl * 256 + f] + bf2f(((const u16*)(ws + WS_B))[s * 256 + f]);
          const float m = bf2f(f2bf(y * __builtin_amdgcn_rcpf(1.f + __builtin_amdgcn_exp2f(y)) * ISCL));
          atomicAdd(&Mf[dl * 256 + f], m);
          cs += m * bf2f(wxg[f]);
        }
        for (int o = 32; o; o >>= 1) cs += __shfl_xor(cs, o);
        if (lane < 3) atomicAdd(&pf[dl * 4 + lane], cs * (lane == 0 ? fx : lane == 1 ? fy : fz));
      }
      __syncthreads();
    }
    for (int i = tid; i < NPB * 256; i += 512) {
      const int n = i >> 8, f = i & 255;
      *(u16*)(smem + O_M + n * 512 + ((f >> 7) << 8) + 16 * (((f >> 3) & 15) ^ (n & 15)) + 2 * (f & 7)) = (u16)f2bf(Mf[i]);
    }
    float pv = 0.f;
    if (tid < 160 && (tid & 31) >= 8) pv = pf[((tid >> 5) * 8 + (tid & 7)) * 4 + ((tid >> 3) & 3) - 1];
    __syncthreads();
    if (tid < 160) { pdp[tid] = pv; pdp[160 + tid] = 0.f; pdp[320 + tid] = 0.f; pdp[480 + tid] = 0.f; }
  }
  {
    const bf16x8* wp = (const bf16x8*)(ws + WS_WHF) + w * 2048 + lane;
    bf16x8 wf[16];
#pragma unroll
    for (int s = 0; s < 16; ++s) wf[s] = wp[s * 64];
    const u32 ba = r31 * 512 + 16 * (hh ^ (r31 & 15));
    const int n8 = lane & 7, u = (lane >> 3) & 1, g4 = lane >> 4;
    const bool act16 = (g4 & 1) == u;
    const u32 bc = (32 + n8) * 512 + 16 * ((g4 >> 1) ^ n8);
    const u32 bch = act16 ? O_H + bc : O_Z, bcm = act16 ? O_M + bc : O_Z;
    f32x16 c32 = {}; f32x4 c16 = {};
#pragma unroll
    for (int s = 0; s < 16; ++s) {
      c32 = MFMA32(wf[s], ld128(smem, O_H + (ba ^ (32 * (s & 7))) + 256 * (s >> 3)), c32);
      c16 = MFMA16(wf[s], ld128(smem, (bch ^ (32 * (s & 7))) + 256 * (s >> 3)), c16);
    }
#pragma unroll
    for (int s = 0; s < 16; ++s) wf[s] = wp[(16 + s) * 64];
    BAR_LGKM();
#pragma unroll
    for (int s = 0; s < 16; ++s) {
      c32 = MFMA32(wf[s], ld128(smem, O_M + (ba ^ (32 * (s & 7))) + 256 * (s >> 3)), c32);
      c16 = MFMA16(wf[s], ld128(smem, (bcm ^ (32 * (s & 7))) + 256 * (s >> 3)), c16);
    }
    auto silu = [](float z) { return z * __builtin_amdgcn_rcpf(1.f + __builtin_amdgcn_exp2f(z * SCL)); };
    {
      const u32 ch = 4 * w + 2 * hh;
      const u32 ha = O_H + r31 * 512 + (ch >> 4) * 256 + 16 * ((ch & 15) ^ (r31 & 15));
      u32x4* o = (u32x4*)(out + (size_t)(node0 + r31) * 256 + 32 * w + 16 * hh);
#pragma unroll
      for (int j = 0; j < 2; ++j) {
        const u32x4 hv = *(const u32x4*)(smem + (ha ^ (16 * j)));
        u32x4 v;
#pragma unroll
        for (int i = 0; i < 4; ++i)
          v[i] = pk2(bf2f(hv[i] & 0xffff) + silu(c32[8 * j + 2 * i]), bf2f(hv[i] >> 16) + silu(c32[8 * j + 2 * i + 1]));
        o[j] = v;
      }
    }
    {
      const u32 f = 32 * w + 16 * (g4 & 1) + 4 * (2 * u + (g4 >> 1)), ch = f >> 3;
      const u32x2 hv = *(const u32x2*)(smem + O_H + (32 + n8) * 512 + (ch >> 4) * 256 + 16 * ((ch & 15) ^ n8) + 2 * (f & 7));
      u32x2 v = {pk2(bf2f(hv[0] & 0xffff) + silu(c16[0]), bf2f(hv[0] >> 16) + silu(c16[1])),
                 pk2(bf2f(hv[1] & 0xffff) + silu(c16[2]), bf2f(hv[1] >> 16) + silu(c16[3]))};
      *(u32x2*)(out + (size_t)(node0 + 32 + n8) * 256 + f) = v;
    }
    if (tid < 3 * NPB) {
      const int n = tid / 3, c = tid - 3 * n, i = (n >> 3) * 32 + 8 * (c + 1) + (n & 7);
      const float d = (pdp[i] + pdp[160 + i]) + (pdp[320 + i] + pdp[480 + i]);
      const u32 pv = ((const u16*)(smem + O_POS))[n * 4 + c];
      out[(size_t)N * 256 + node0 * 3 + tid] = (u16)f2bf(bf2f(pv) + d);
    }
  }
}

extern "C" void kernel_launch(void* const* d_in, const int* in_sizes, int n_in, void* d_out, int out_size, void* d_ws, size_t ws_size,
                              hipStream_t stream) {
  const u16* h = (const u16*)d_in[0];
  const u16* pos = (const u16*)d_in[1];
  const u16* ea = (const u16*)d_in[2];
  const int* src = (const int*)d_in[3];
  const int* dst = (const int*)d_in[4];
  const u16* we = (const u16*)d_in[5];
  const u16* wx = (const u16*)d_in[6];
  const u16* wh = (const u16*)d_in[7];
  k1<<<253, 512, 0, stream>>>(h, pos, src, dst, we, wx, wh, (char*)d_ws);
  k2<<<NB, 512, 0, stream>>>(h, ea, src, dst, we, wx, (const char*)d_ws, (u16*)d_out);
}
```

```cpp
#include <hip/hip_runtime.h>
#include <stdint.h>

typedef unsigned short u16;
typedef unsigned int u32;
typedef short bf16x8 __attribute__((ext_vector_type(8)));
typedef short s16x4 __attribute__((ext_vector_type(4)));
typedef float f32x16 __attribute__((ext_vector_type(16)));
typedef float f32x4 __attribute__((ext_vector_type(4)));
typedef float f32x2 __attribute__((ext_vector_type(2)));
typedef __bf16 bf2 __attribute__((ext_vector_type(2)));
typedef u32 u32x4 __attribute__((ext_vector_type(4)));
typedef u32 u32x2 __attribute__((ext_vector_type(2)));
typedef __attribute__((address_space(3))) s16x4* lds_s16x4_p;
typedef __attribute__((address_space(3))) char* lds_char_p;
#define DI __device__ __forceinline__
#define MFMA32(a, b, c) __builtin_amdgcn_mfma_f32_32x32x16_bf16((a), (b), (c), 0, 0, 0)
#define MFMA16(a, b, c) __builtin_amdgcn_mfma_f32_16x16x32_bf16((a), (b), (c), 0, 0, 0)
#define SB() __builtin_amdgcn_sched_barrier(0)

constexpr int N = 10000, E = 160000;
constexpr int NPB = 40, NB = 250;
constexpr int NCH = 64, CHE = 2500;
constexpr int TCAP = 26;
constexpr float SCL = -1.4426950408889634f;
constexpr float ISCL = -0.6931471805599453f;
constexpr u32 OOB = 0x40000000u;

constexpr size_t WS_B = 0;
constexpr size_t WS_CELL = 5120000;
constexpr size_t WS_W1F = WS_CELL + (size_t)NB * NCH * 128;
constexpr size_t WS_WHF = WS_W1F + 131072;
constexpr size_t WS_W4F = WS_WHF + 262144;
constexpr size_t WS_WDP = WS_W4F + 8192;
constexpr size_t WS_WXF = WS_WDP + 1024;
constexpr size_t WS_POS4 = WS_WXF + 512;

DI float bf2f(u32 x) { return __uint_as_float(x << 16); }
DI u32 pk2(float a, float b) { f32x2 v = {a, b}; return __builtin_bit_cast(u32, __builtin_convertvector(v, bf2)); }
DI u32 f2bf(float a) { return pk2(a, 0.f) & 0xffffu; }
DI u32 lds_addr(const void* p) { return (u32)(uintptr_t)(lds_char_p)p; }
DI __amdgpu_buffer_rsrc_t mk_rsrc(const void* p, u32 bytes) { return __builtin_amdgcn_make_buffer_rsrc((void*)p, (short)0, (int)bytes, 0x00020000); }
DI void dma16(u32 m0v, u32 voff, __amdgpu_buffer_rsrc_t r, u32 soff) {
  asm volatile("s_nop 4\n\ts_mov_b32 m0, %0\n\ts_nop 0\n\tbuffer_load_dwordx4 %1, %2, %3 offen lds" ::"s"(m0v), "v"(voff), "s"(r), "s"(soff) : "memory");
}
DI void dma16f(u32 m0v, u32 voff, __amdgpu_buffer_rsrc_t r, u32 soff) {
  asm volatile("s_mov_b32 m0, %0\n\ts_nop 0\n\tbuffer_load_dwordx4 %1, %2, %3 offen lds" ::"s"(m0v), "v"(voff), "s"(r), "s"(soff) : "memory");
}
#define BAR_LGKM() asm volatile("s_waitcnt lgkmcnt(0)\n\ts_barrier" ::: "memory")
#define BAR_ALL() asm volatile("s_waitcnt vmcnt(0) lgkmcnt(0)\n\ts_barrier" ::: "memory")
DI bf16x8 ld128(const char* smem, u32 off) { return *(const bf16x8*)(smem + off); }
DI bf16x8 tr2(const char* smem, u32 o0, u32 o1) {
  s16x4 a = __builtin_amdgcn_ds_read_tr16_b64_v4i16((lds_s16x4_p)(lds_char_p)(smem + o0));
  s16x4 b = __builtin_amdgcn_ds_read_tr16_b64_v4i16((lds_s16x4_p)(lds_char_p)(smem + o1));
  return __builtin_shufflevector(a, b, 0, 1, 2, 3, 4, 5, 6, 7);
}
DI bf16x8 asbf(u32x4 v) { return __builtin_bit_cast(bf16x8, v); }

#define STAMP_DECL
#define STAMP(idx)
#define STAMP_FLUSH(kern)
__global__ __launch_bounds__(512) void k1(const u16* __restrict__ h, const u16* __restrict__ pos, const int* __restrict__ src,
                                          const int* __restrict__ dst, const u16* __restrict__ we, const u16* __restrict__ wx,
                                          const u16* __restrict__ wh, char* __restrict__ ws) {
  __shared__ __attribute__((aligned(1024))) char smem[131072];
  const int bid = blockIdx.x, tid = threadIdx.x, lane = tid & 63;
  const int w = __builtin_amdgcn_readfirstlane(tid >> 6);
  STAMP_DECL; STAMP(0);
  if (bid < 160) {
    const int tile = (bid >> 4) * 8 + (bid & 7), half = (bid >> 3) & 1;
    if (tile >= 79) return;
    const int node0 = tile * 128;
    const u32 sb = lds_addr(smem);
    const __amdgpu_buffer_rsrc_t rW = mk_rsrc(we + 256 * 256, 256 * 512), rH = mk_rsrc(h, N * 512);
    {
      const u32 rq = lane >> 4;
      const u32 vW = (8 * w + rq) * 512 + half * 256 + 16 * ((lane & 15) ^ (((rq & 1) << 1) | ((rq & 2) << 2)));
      const u32 hr = node0 + 16 * w + (lane >> 3);
      const u32 vH0 = hr * 512 + 16 * ((lane & 7) ^ rq), vH1 = (hr + 8) * 512 + 16 * ((lane & 7) ^ (4 + rq));
#pragma unroll
      for (int st = 0; st < 4; ++st) {
        dma16(sb + st * 16384 + w * 2048, vW, rW, st * 32768);
        dma16(sb + st * 16384 + w * 2048 + 1024, vW, rW, st * 32768 + 2048);
        dma16(sb + 65536 + st * 16384 + w * 2048, vH0, rH, st * 128);
        dma16(sb + 65536 + st * 16384 + w * 2048 + 1024, vH1, rH, st * 128);
      }
    }
    const int fq = w & 1, nt = w >> 1, hh = lane >> 5;
    u32 aw[2];
    {
      const u32 blk = (lane >> 4) & 1, q = (lane & 15) >> 2, p = lane & 3, u = 4 * blk + p;
      const u32 fw = ((q & 1) << 1) | ((q & 2) << 2);
#pragma unroll
      for (int m = 0; m < 2; ++m) {
        const u32 v = 8 * (u & 1) + 4 * m + (u >> 1), ch = 8 * fq + (v >> 1);
        aw[m] = 256 * (8 * hh + q) + 16 * (ch ^ fw) + 8 * (v & 1);
      }
    }
    const u32 hrow = 32 * nt + (lane & 31);
    const u32 bh = 65536 + hrow * 128 + 16 * (hh ^ ((hrow >> 1) & 7));
    f32x16 acc0 = {}, acc1 = {};
#pragma unroll
    for (int st = 0; st < 4; ++st) {
      if (st == 0) asm volatile("s_waitcnt vmcnt(12)\n\ts_barrier" ::: "memory");
      if (st == 1) asm volatile("s_waitcnt vmcnt(8)\n\ts_barrier" ::: "memory");
      if (st == 2) asm volatile("s_waitcnt vmcnt(4)\n\ts_barrier" ::: "memory");
      if (st == 3) asm volatile("s_waitcnt vmcnt(0)\n\ts_barrier" ::: "memory");
      STAMP(1 + st);
#pragma unroll
      for (int kk = 0; kk < 4; ++kk) {
        const int ks = 4 * st + kk;
        const bf16x8 a0 = tr2(smem, aw[0] + 4096 * ks, aw[0] + 4096 * ks + 1024);
        const bf16x8 a1 = tr2(smem, aw[1] + 4096 * ks, aw[1] + 4096 * ks + 1024);
        const bf16x8 b = ld128(smem, (bh ^ (32 * kk)) + 16384 * st);
        acc0 = MFMA32(a0, b, acc0);
        acc1 = MFMA32(a1, b, acc1);
      }
    }
    STAMP(5);
    const int node = node0 + hrow;
    if (node < N) {
      u32x4* o = (u32x4*)(ws + WS_B + (size_t)node * 512 + half * 256 + fq * 128 + hh * 64);
      u32x4 v;
#pragma unroll
      for (int j = 0; j < 2; ++j) {
#pragma unroll
        for (int i = 0; i < 4; ++i) v[i] = pk2(acc0[8 * j + 2 * i] * SCL, acc0[8 * j + 2 * i + 1] * SCL);
        o[j] = v;
      }
#pragma unroll
      for (int j = 0; j < 2; ++j) {
#pragma unroll
        for (int i = 0; i < 4; ++i) v[i] = pk2(acc1[8 * j + 2 * i] * SCL, acc1[8 * j + 2 * i + 1] * SCL);
        o[2 + j] = v;
      }
    }
  } else if (bid < 224) {
    const int c = bid - 160;
    u32* cnt = (u32*)smem;
    u32* stage = (u32*)(smem + 1024);
    const int4* d4 = (const int4*)(dst + c * CHE);
    const int4* s4 = (const int4*)(src + c * CHE);
    const bool has2 = tid < CHE / 4 - 512;
    const int i2 = has2 ? 512 + tid : tid;
    const int4 dv0 = d4[tid], sv0 = s4[tid], dv1 = d4[i2], sv1 = s4[i2];
    if (tid < 256) cnt[tid] = 0;
    __syncthreads();
    asm volatile("s_waitcnt vmcnt(0)" ::: "memory");
    STAMP(1);
    auto put = [&](int d, int s, int el) {
      const u32 b = (u32)d / NPB, dl = d - b * NPB;
      const u32 r = atomicAdd(&cnt[b], 1u);
      if (r < 31) stage[b * 36 + 1 + r] = (dl << 26) | ((u32)s << 12) | (u32)el;
    };
    put(dv0.x, sv0.x, 4 * tid); put(dv0.y, sv0.y, 4 * tid + 1); put(dv0.z, sv0.z, 4 * tid + 2); put(dv0.w, sv0.w, 4 * tid + 3);
    if (has2) { put(dv1.x, sv1.x, 4 * i2); put(dv1.y, sv1.y, 4 * i2 + 1); put(dv1.z, sv1.z, 4 * i2 + 2); put(dv1.w, sv1.w, 4 * i2 + 3); }
    __syncthreads();
    STAMP(2);
    u32x4* cells = (u32x4*)(ws + WS_CELL);
#pragma unroll
    for (int it = 0; it < 4; ++it) {
      const int i = tid + 512 * it;
      if (i < NB * 8) {
        const int b = i >> 3, pc = i & 7;
        u32x4 v = *(const u32x4*)&stage[b * 36 + 4 * pc];
        if (pc == 0) v[0] = cnt[b];
        cells[(b * NCH + c) * 8 + pc] = v;
      }
    }
  } else if (bid < 248) {
#pragma unroll
    for (int it = 0; it < 2; ++it) {
      const int x = (bid - 224) * 1024 + 512 * it + tid;
      const u16* p;
      if (x < 8192) {
        const int ct = x >> 10, s = (x >> 6) & 15;
        p = we + (16 * s + 8 * (lane >> 5)) * 256 + 32 * ct + (lane & 31);
      } else {
        const int y = x - 8192, mt = y >> 11, s = (y >> 6) & 31, r = lane & 31;
        p = wh + (16 * s + 8 * (lane >> 5)) * 256 + 32 * mt + 16 * ((r >> 2) & 1) + 4 * (r >> 3) + (r & 3);
      }
      u32x4 v;
#pragma unroll
      for (int j = 0; j < 4; ++j) v[j] = (u32)p[(2 * j) * 256] | ((u32)p[(2 * j + 1) * 256] << 16);
      *(u32x4*)(ws + WS_W1F + (size_t)x * 16) = v;
    }
  } else if (bid == 248) {
    {
      const u16* p = we + (513 + 8 * (lane >> 5)) * 256 + 32 * w + (lane & 31);
      u32x4 v;
#pragma unroll
      for (int j = 0; j < 4; ++j) v[j] = pk2(bf2f(p[(2 * j) * 256]) * SCL, bf2f(p[(2 * j + 1) * 256]) * SCL);
      *(u32x4*)(ws + WS_W4F + tid * 16) = v;
    }
    if (tid < 256) {
      const float v = bf2f(we[512 * 256 + tid]) * SCL;
      const u32 hi = f2bf(v);
      ((u32*)(ws + WS_WDP))[tid] = hi | (f2bf(v - bf2f(hi)) << 16);
    }
    if (tid < 32) {
      const int f0 = 32 * (tid >> 2) + 16 * ((tid >> 1) & 1) + 4 * (tid & 1);
      u32x4 v;
      v[0] = (u32)wx[f0] | ((u32)wx[f0 + 1] << 16); v[1] = (u32)wx[f0 + 2] | ((u32)wx[f0 + 3] << 16);
      v[2] = (u32)wx[f0 + 8] | ((u32)wx[f0 + 9] << 16); v[3] = (u32)wx[f0 + 10] | ((u32)wx[f0 + 11] << 16);
      *(u32x4*)(ws + WS_WXF + tid * 16) = v;
    }
  } else {
    const int n0 = (bid - 249) * 2500;
#pragma unroll
    for (int it = 0; it < 5; ++it) {
      const int i = tid + 512 * it;
      if (i < 2500) {
        const u16* p = pos + (n0 + i) * 3;
        u32x2 v = {(u32)p[0] | ((u32)p[1] << 16), (u32)p[2]};
        *(u32x2*)(ws + WS_POS4 + (size_t)(n0 + i) * 8) = v;
      }
    }
  }
  STAMP(10);
  asm volatile("s_waitcnt vmcnt(0)" ::: "memory");
  STAMP_FLUSH(0);
}

constexpr u32 LT = 2112;
constexpr u32 O_L = 0;
constexpr u32 O_A1 = O_L + TCAP * LT;
constexpr u32 O_SRC = O_A1 + TCAP * 1024;
constexpr u32 O_EL = O_SRC + (TCAP + 1) * 128;
constexpr u32 O_G = 88320;
constexpr u32 O_H = O_G + 32768;
constexpr u32 O_M = O_H + 20480;
constexpr u32 O_CNT = O_M + 20480;
constexpr u32 O_POS = O_CNT + 64;
constexpr u32 O_WX = O_POS + 320;
constexpr u32 O_Z = O_WX + 512;
constexpr u32 LDS2 = O_Z + 512;

struct Frag { bf16x8 g0, g1, g2, g3, a1, a2; };

template <int NBLK_STATIC>
DI void act(const f32x16& x, u32x4& p0, u32x4& p1, int nblk) {
#pragma unroll
  for (int b = 0; b < 4; ++b) {
    u32 r0 = 0, r1 = 0;
    if (b < nblk) {
      f32x2 y0 = {x[4 * b], x[4 * b + 1]}, y1 = {x[4 * b + 2], x[4 * b + 3]}, e0, e1;
      e0[0] = __builtin_amdgcn_exp2f(y0[0]); e0[1] = __builtin_amdgcn_exp2f(y0[1]);
      e1[0] = __builtin_amdgcn_exp2f(y1[0]); e1[1] = __builtin_amdgcn_exp2f(y1[1]);
      e0 = e0 + 1.0f; e1 = e1 + 1.0f;
      e0[0] = __builtin_amdgcn_rcpf(e0[0]); e0[1] = __builtin_amdgcn_rcpf(e0[1]);
      e1[0] = __builtin_amdgcn_rcpf(e1[0]); e1[1] = __builtin_amdgcn_rcpf(e1[1]);
      e0 = e0 * y0; e1 = e1 * y1;
      r0 = pk2(e0[0], e0[1]); r1 = pk2(e1[0], e1[1]);
    }
    if (b < 2) { p0[2 * b] = r0; p0[2 * b + 1] = r1; } else { p1[2 * b - 4] = r0; p1[2 * b - 3] = r1; }
  }
}

template <class M1, class M2, class M3, class M4, class M5, class M6, class MID, class LATE>
DI void stage(const f32x16& x, u32x4& p0, u32x4& p1, M1 m1, M2 m2, M3 m3, M4 m4, M5 m5, M6 m6, MID mid, LATE late) {
  f32x2 e[8];
#define EX(i) e[i][0] = __builtin_amdgcn_exp2f(x[2 * (i)]); e[i][1] = __builtin_amdgcn_exp2f(x[2 * (i) + 1]);
#define RC(i) e[i][0] = __builtin_amdgcn_rcpf(e[i][0]); e[i][1] = __builtin_amdgcn_rcpf(e[i][1]);
#define PIN2(i, j) asm volatile("" : "+v"(e[i]), "+v"(e[j])); SB();
#define PIN1(i) asm volatile("" : "+v"(e[i])); SB();
  SB(); m1(); SB(); EX(0) EX(1) PIN2(0, 1) m2(); SB(); EX(2) EX(3) PIN2(2, 3) m3(); SB(); EX(4) EX(5) PIN2(4, 5) m4(); SB(); EX(6) EX(7) PIN2(6, 7)
#pragma unroll
  for (int i = 0; i < 8; ++i) e[i] = e[i] + 1.0f;
  asm volatile("" : "+v"(e[0]), "+v"(e[1]), "+v"(e[2]), "+v"(e[3]), "+v"(e[4]), "+v"(e[5]), "+v"(e[6]), "+v"(e[7]));
  SB(); mid(); SB();
  RC(0) PIN1(0) m5(); SB(); RC(1) RC(2) PIN2(1, 2) m6(); SB(); RC(3) PIN1(3) late(); SB(); RC(4) RC(5) RC(6) RC(7)
  asm volatile("" : "+v"(e[4]), "+v"(e[5]), "+v"(e[6]), "+v"(e[7])); SB();
#pragma unroll
  for (int i = 0; i < 8; ++i) { f32x2 y = {x[2 * i], x[2 * i + 1]}; e[i] = e[i] * y; }
  SB();
#pragma unroll
  for (int i = 0; i < 4; ++i) { p0[i] = pk2(e[i][0], e[i][1]); p1[i] = pk2(e[4 + i][0], e[4 + i][1]); }
  asm volatile("" : "+v"(p0), "+v"(p1));
  SB();
#undef PIN2
#undef PIN1
#undef EX
#undef RC
}

__global__ __launch_bounds__(512) void k2(const u16* __restrict__ h, const u16* __restrict__ ea, const int* __restrict__ srcg,
                                          const int* __restrict__ dstg, const u16* __restrict__ we, const u16* __restrict__ wxg,
                                          const char* __restrict__ ws, u16* __restrict__ out) {
  __shared__ __attribute__((aligned(1024))) char smem[LDS2];
  const int tid = threadIdx.x, lane = tid & 63, hh = lane >> 5, r31 = lane & 31;
  const int w = __builtin_amdgcn_readfirstlane(tid >> 6), q = w & 3, p = w >> 2;
  const int b = blockIdx.x, node0 = b * NPB;
  const u32 sb = lds_addr(smem);
  const __amdgpu_buffer_rsrc_t rH = mk_rsrc(h, N * 512), rB = mk_rsrc(ws + WS_B, N * 512), rE = mk_rsrc(ea, E * 32);

  STAMP_DECL; STAMP(0);
  {
    const u32 row = 2 * w + hh;
    const u32 vh = (node0 + row) * 512 + 16 * (r31 ^ (row & 15));
    dma16(sb + O_H + w * 1024, vh, rH, 0);
    dma16(sb + O_H + w * 1024 + 8192, vh, rH, 8192);
    if (w < 4) dma16(sb + O_H + w * 1024 + 16384, vh, rH, 16384);
  }
  const u32x4 cw = *(const u32x4*)(ws + WS_CELL + (size_t)b * (NCH * 128) + tid * 16);
  bf16x8 w1[16];
  {
    const bf16x8* wp = (const bf16x8*)(ws + WS_W1F) + (2 * q + p) * 1024 + lane;
#pragma unroll
    for (int s = 0; s < 16; ++s) w1[s] = wp[s * 64];
  }
  bf16x8 W40 = *(const bf16x8*)(ws + WS_W4F + (2 * q) * 1024 + lane * 16), W41 = *(const bf16x8*)(ws + WS_W4F + (2 * q + 1) * 1024 + lane * 16);
  u32 wd0 = ((const u32*)(ws + WS_WDP))[64 * q + r31], wd1 = ((const u32*)(ws + WS_WDP))[64 * q + 32 + r31];
  const u32x4 small = *(const u32x4*)(ws + WS_WXF + (tid < 32 ? tid * 16 : 512 + node0 * 8 + ((tid - 32) & 15) * 16 + (tid >= 48 && tid < 52 ? 256 : 0)));
  SB();
  {
    const u32x4 z = {0, 0, 0, 0}, oo = {OOB, OOB, OOB, OOB}, ff = {~0u, ~0u, ~0u, ~0u};
#pragma unroll
    for (int i = 0; i < 7; ++i) { const u32 o = (tid + 512 * i) * 16; if (o < O_A1) *(u32x4*)(smem + o) = z; }
    constexpr int n1 = TCAP * 8 + 8, n2 = n1 + TCAP * 8;
    if (tid < n2 + 36) *(u32x4*)(smem + (tid < n2 ? O_SRC + tid * 16 : tid < n2 + 4 ? O_CNT + (tid - n2) * 16 : O_Z + (tid - n2 - 4) * 16)) = tid < n1 ? oo : tid < n2 ? ff : z;
  }
  BAR_LGKM();
  STAMP(1);
  u32* gcnt = (u32*)(smem + O_CNT);
  const u32 ccnt = (u32)__builtin_amdgcn_ds_swizzle((int)cw[0], 0x18);
  STAMP(2);
  u32 rank[4];
  const u32 i8 = (tid & 7) * 4;
#pragma unroll
  for (int k = 0; k < 4; ++k) {
    rank[k] = ~0u;
    if (i8 + k >= 1 && i8 + k <= ccnt) rank[k] = atomicAdd(&gcnt[cw[k] >> 29], 1u);
  }
  if (ccnt > 31) gcnt[7] = 1;
  if (tid < 32) *(u32x4*)(smem + O_WX + tid * 16) = small;
  else if (tid < 52) *(u32x4*)(smem + O_POS + (tid - 32) * 16) = small;
  BAR_LGKM();
  int ng[5], tb[6];
  bool slow;
  {
    const u32x4 c0 = *(const u32x4*)(smem + O_CNT), c1 = *(const u32x4*)(smem + O_CNT + 16);
    ng[0] = __builtin_amdgcn_readfirstlane(c0[0]); ng[1] = __builtin_amdgcn_readfirstlane(c0[1]); ng[2] = __builtin_amdgcn_readfirstlane(c0[2]);
    ng[3] = __builtin_amdgcn_readfirstlane(c0[3]); ng[4] = __builtin_amdgcn_readfirstlane(c1[0]);
    tb[0] = 0;
#pragma unroll
    for (int g = 0; g < 5; ++g) tb[g + 1] = tb[g] + ((ng[g] + 31) >> 5);
    slow = __builtin_amdgcn_readfirstlane(c1[3]) != 0 || tb[5] > TCAP;
  }
  float* pdp = (float*)(smem + O_EL);
  if (!slow) {
    {
      const u32 tbp = (u32)tb[1] << 6 | (u32)tb[2] << 12 | (u32)tb[3] << 18 | (u32)tb[4] << 24;
      const u32 ebase = (tid >> 3) * CHE;
#pragma unroll
      for (int k = 0; k < 4; ++k)
        if (rank[k] != ~0u) {
          const u32 x = cw[k], slot = (((tbp >> (6 * (x >> 29))) & 63) << 5) + rank[k];
          *(u32*)(smem + O_EL + slot * 4) = (ebase + (x & 4095)) | ((x >> 26) << 18);
          *(u32*)(smem + O_SRC + (slot >> 5) * 128 + (slot & 7) * 16 + ((slot >> 3) & 3) * 4) = ((x >> 12) & 0x3fff) << 9;
        }
    }
    BAR_ALL();
    STAMP(3);
    const u32 gsw0 = 16 * ((lane & 7) ^ (lane >> 4)), gsw1 = gsw0 ^ 64;
    const u32 gm0 = sb + O_G + w * 4096, gso = q * 128;
    const u32 srd = O_SRC + (lane >> 3) * 16;
    auto gdma = [&](u32x4 s) {
      dma16f(gm0, s[0] + gsw0, rB, gso); dma16f(gm0 + 1024, s[1] + gsw1, rB, gso);
      dma16f(gm0 + 2048, s[2] + gsw0, rB, gso); dma16f(gm0 + 3072, s[3] + gsw1, rB, gso);
    };
    const int t0 = p ? tb[2] : 0, t1 = p ? tb[4] : tb[2], t2 = p ? tb[5] : tb[4], t3 = tb[5];
    auto nextt = [&](int t) { return t + 1 == t1 ? t2 : t + 1; };
    {
      const int tf = t0 < t1 ? t0 : t2;
      gdma(*(const u32x4*)(smem + srd + (tf < t3 ? tf : TCAP) * 128));
    }
    u32 en[2]; u32x2 ps[2];
    {
      u32 ee[4], so[2];
#pragma unroll
      for (int i = 0; i < 4; ++i) { const int t = w + 8 * i; ee[i] = *(const u32*)(smem + O_EL + ((t < TCAP ? t : 0) * 32 + r31) * 4); }
#pragma unroll
      for (int i = 0; i < 2; ++i) {
        const u32 s0 = tid + 512 * i, s = s0 < TCAP * 32 ? s0 : 0;
        en[i] = *(const u32*)(smem + O_EL + s * 4);
        so[i] = *(const u32*)(smem + O_SRC + (s >> 5) * 128 + (s & 7) * 16 + ((s >> 3) & 3) * 4);
        if (s0 >= TCAP * 32) en[i] = ~0u;
      }
      const __amdgpu_buffer_rsrc_t rP = mk_rsrc(ws + WS_POS4, N * 8);
#pragma unroll
      for (int i = 0; i < 2; ++i) ps[i] = __builtin_bit_cast(u32x2, __builtin_amdgcn_raw_buffer_load_b64(rP, en[i] == ~0u ? OOB : so[i] >> 6, 0, 0));
#pragma unroll
      for (int i = 0; i < 4; ++i) {
        const int t = w + 8 * i;
        if (t < t3) dma16f(sb + O_A1 + t * 1024, ee[i] == ~0u ? OOB : (ee[i] & 0x3ffff) * 32 + 16 * hh, rE, 0);
      }
    }
    SB();
    STAMP(4);
    u32x4 own0, own1;
    {
      const int blk = r31 >> 4, gh = (r31 >> 2) & 1, j = (r31 & 3) + 4 * ((r31 >> 3) & 1);
      const u32 na = blk == 0 ? 8 * gh + j : 32 + j, nb2 = 16 + 8 * gh + j;
      const u32 aa = O_H + na * 512 + 16 * (hh ^ (na & 15)), ab = O_H + nb2 * 512 + 16 * (hh ^ (nb2 & 15));
      f32x16 ca = {}, cb = {};
#pragma unroll
      for (int s = 0; s < 16; ++s) {
        ca = MFMA32(ld128(smem, (aa ^ (32 * (s & 7))) + 256 * (s >> 3)), w1[s], ca);
        cb = MFMA32(ld128(smem, (ab ^ (32 * (s & 7))) + 256 * (s >> 3)), w1[s], cb);
      }
      u32x4 pa, pb, pc;
#pragma unroll
      for (int i = 0; i < 4; ++i) {
        pa[i] = pk2(ca[2 * i] * SCL, ca[2 * i + 1] * SCL);
        pb[i] = pk2(ca[8 + 2 * i] * SCL, ca[9 + 2 * i] * SCL);
        pc[i] = pk2(cb[2 * i] * SCL, cb[2 * i + 1] * SCL);
      }
      u32x4* ex = (u32x4*)(smem + O_M + q * 3072) + lane;
      if (p == 0) { ex[0] = pc; own0 = pa; own1 = pb; }
      else { ex[64] = pa; ex[128] = pb; own0 = pc; own1 = pc; }
    }
    SB();
    STAMP(5);
    asm volatile("" : "+v"(ps[0]), "+v"(ps[1]));
#pragma unroll
    for (int i = 0; i < 2; ++i)
      if (en[i] != ~0u) {
        const u32 s = tid + 512 * i, dl = en[i] >> 18;
        const u32x2 pd = *(const u32x2*)(smem + O_POS + dl * 8);
        const u32 dxy = pk2(bf2f(pd[0] & 0xffff) - bf2f(ps[i][0] & 0xffff), bf2f(pd[0] >> 16) - bf2f(ps[i][0] >> 16));
        const u32 dz = f2bf(bf2f(pd[1] & 0xffff) - bf2f(ps[i][1] & 0xffff));
        const float fx = bf2f(dxy & 0xffff), fy = bf2f(dxy >> 16), fz = bf2f(dz);
        const u32 sxy = pk2(fx * fx, fy * fy), sz = f2bf(fz * fz);
        const u32 d2 = f2bf(bf2f(sxy & 0xffff) + bf2f(sxy >> 16) + bf2f(sz));
        u16* lp = (u16*)(smem + O_L + (s >> 5) * LT + ((s >> 4) & 1) * 1056 + ((s >> 2) & 1) * 528 + 2 * ((s & 3) + 4 * ((s >> 3) & 1)));
        const u32 n = dl & 7;
        lp[n * 8] = 0x3f80; lp[64 + n * 8] = (u16)dxy; lp[128 + n * 8] = (u16)(dxy >> 16); lp[192 + n * 8] = (u16)dz; lp[256] = (u16)d2;
      }
    BAR_ALL();
    u32x4 PA0, PA1, PB0, PB1;
    {
      const u32x4* ex = (const u32x4*)(smem + O_M + q * 3072) + lane;
      if (p == 0) { PA0 = own0; PB0 = own1; PA1 = ex[64]; PB1 = ex[128]; }
      else { PA0 = ex[0]; PA1 = own0; PB0 = PA0; PB1 = PA1; }
    }
    BAR_LGKM();
    STAMP(6);
    asm volatile("" : "+v"(W40), "+v"(W41), "+v"(wd0), "+v"(wd1));
    bf16x8 I0, I1;
    {
      u32x4 a, c;
#pragma unroll
      for (int i = 0; i < 4; ++i) {
        const int k = 8 * hh + 2 * i;
        a[i] = (r31 == k ? 0x3f80u : 0u) | (r31 == k + 1 ? 0x3f800000u : 0u);
        c[i] = (r31 == k + 16 ? 0x3f80u : 0u) | (r31 == k + 17 ? 0x3f800000u : 0u);
      }
      I0 = asbf(a); I1 = asbf(c);
    }
    const u32 ga = O_G + w * 4096 + r31 * 128 + 16 * (hh ^ ((r31 >> 1) & 7));
    const u32 la = O_L + hh * 528 + r31 * 16, a1a = O_A1 + lane * 16;
    const u32 tra = O_L + ((lane >> 4) & 1) * 1056 + (lane & 1) * 528 + 8 * ((lane >> 1) & 1);
    const int ngr = p ? 2 : 3;
    for (int gi = 0; gi < ngr; ++gi) {
      const int g = gi == 2 ? 4 : 2 * p + gi;
      const int n = g == 0 ? ng[0] : g == 1 ? ng[1] : g == 2 ? ng[2] : g == 3 ? ng[3] : ng[4];
      const int ts = g == 0 ? tb[0] : g == 1 ? tb[1] : g == 2 ? tb[2] : g == 3 ? tb[3] : tb[4];
      const int te = ts + ((n + 31) >> 5);
      const bool oh = hh == (gi & 1);
      bf16x8 B20, B21;
      {
        const u32x4 s0 = gi == 2 ? PB0 : PA0, s1 = gi == 2 ? PB1 : PA1;
        u32x4 a = {oh ? s0[0] : wd0, oh ? s0[1] : 0u, oh ? s0[2] : 0u, oh ? s0[3] : 0u};
        u32x4 c = {oh ? s1[0] : wd1, oh ? s1[1] : 0u, oh ? s1[2] : 0u, oh ? s1[3] : 0u};
        B20 = asbf(a); B21 = asbf(c);
      }
      const u32 tr0 = tra + (oh ? ((lane >> 2) & 3) * 16 : 512), tr1 = tra + (oh ? ((lane >> 2) & 3) * 16 + 64 : 512);
      f32x16 Y0 = {}, Y1 = {};
      if (n > 0) {
        Frag f; u32x4 ns;
        auto rdfrag = [&](int t) {
          asm volatile("s_waitcnt vmcnt(0)" ::: "memory");
          f.g0 = ld128(smem, ga); f.g1 = ld128(smem, ga ^ 32); f.a1 = ld128(smem, a1a + t * 1024); f.a2 = tr2(smem, tr0 + t * LT, tr1 + t * LT);
          f.g2 = ld128(smem, ga ^ 64); f.g3 = ld128(smem, ga ^ 96);
          const int tn = nextt(t);
          ns = *(const u32x4*)(smem + srd + (tn < t3 ? tn : TCAP) * 128);
        };
        auto issue = [&](int t) {
          asm volatile("s_waitcnt lgkmcnt(0)" ::: "memory");
          gdma(ns);
        };
        f32x16 X0, X1; u32x4 P00, P01, P10 = {0, 0, 0, 0}, P11 = {0, 0, 0, 0};
        bf16x8 L0, L1;
        const f32x16 zz = {};
        rdfrag(ts); issue(ts);
        L0 = ld128(smem, la + ts * LT); L1 = L0;
        X0 = MFMA32(f.a1, W40, zz); X0 = MFMA32(f.a2, B20, X0); X0 = MFMA32(f.g0, I0, X0); X0 = MFMA32(f.g1, I1, X0);
        for (int t = ts; t < te - 1; ++t) {
          stage(X0, P00, P01,
                [&] { X1 = MFMA32(f.a1, W41, zz); }, [&] { X1 = MFMA32(f.a2, B21, X1); }, [&] { X1 = MFMA32(f.g2, I0, X1); }, [&] { X1 = MFMA32(f.g3, I1, X1); },
                [&] { Y1 = MFMA32(asbf(P10), L0, Y1); }, [&] { Y1 = MFMA32(asbf(P11), L1, Y1); },
                [&] { rdfrag(t + 1); },
                [&] { L0 = ld128(smem, la + t * LT); L1 = ld128(smem, la + t * LT + 1056); });
          issue(t + 1);
          stage(X1, P10, P11,
                [&] { X0 = MFMA32(f.a1, W40, zz); }, [&] { X0 = MFMA32(f.a2, B20, X0); }, [&] { X0 = MFMA32(f.g0, I0, X0); }, [&] { X0 = MFMA32(f.g1, I1, X0); },
                [&] { Y0 = MFMA32(asbf(P00), L0, Y0); }, [&] { Y0 = MFMA32(asbf(P01), L1, Y0); },
                [&] {}, [&] {});
        }
        {
          const int t = te - 1, nblk = (n - 32 * (t - ts) + 7) >> 3;
          X1 = MFMA32(f.a1, W41, zz); Y1 = MFMA32(asbf(P10), L0, Y1); X1 = MFMA32(f.a2, B21, X1); Y1 = MFMA32(asbf(P11), L1, Y1);
          X1 = MFMA32(f.g2, I0, X1); X1 = MFMA32(f.g3, I1, X1);
          L0 = ld128(smem, la + t * LT); L1 = ld128(smem, la + t * LT + 1056);
          act<0>(X0, P00, P01, nblk);
          Y0 = MFMA32(asbf(P00), L0, Y0);
          if (nblk > 2) Y0 = MFMA32(asbf(P01), L1, Y0);
          act<0>(X1, P10, P11, nblk);
          Y1 = MFMA32(asbf(P10), L0, Y1);
          if (nblk > 2) Y1 = MFMA32(asbf(P11), L1, Y1);
        }
      }
      {
        u32x4 k00, k01, k10, k11;
#pragma unroll
        for (int i = 0; i < 4; ++i) {
          k00[i] = pk2(Y0[2 * i] * ISCL, Y0[2 * i + 1] * ISCL); k01[i] = pk2(Y0[8 + 2 * i] * ISCL, Y0[9 + 2 * i] * ISCL);
          k10[i] = pk2(Y1[2 * i] * ISCL, Y1[2 * i + 1] * ISCL); k11[i] = pk2(Y1[8 + 2 * i] * ISCL, Y1[9 + 2 * i] * ISCL);
        }
        const u32 wxa = O_WX + q * 128 + hh * 16;
        f32x16 Z = {};
        Z = MFMA32(ld128(smem, wxa), asbf(k00), Z); Z = MFMA32(ld128(smem, wxa + 32), asbf(k01), Z);
        Z = MFMA32(ld128(smem, wxa + 64), asbf(k10), Z); Z = MFMA32(ld128(smem, wxa + 96), asbf(k11), Z);
        if (r31 < 8) {
          const u32 nd = 8 * g + r31;
          const u32 ma = O_M + nd * 512 + 8 * hh + (q >> 1) * 256 + 16 * (((q & 1) * 8) ^ (nd & 15));
          u32x2 v;
          v = u32x2{k00[0], k00[1]}; *(u32x2*)(smem + ma) = v;         v = u32x2{k00[2], k00[3]}; *(u32x2*)(smem + (ma ^ 16)) = v;
          v = u32x2{k01[0], k01[1]}; *(u32x2*)(smem + (ma ^ 32)) = v;  v = u32x2{k01[2], k01[3]}; *(u32x2*)(smem + (ma ^ 48)) = v;
          v = u32x2{k10[0], k10[1]}; *(u32x2*)(smem + (ma ^ 64)) = v;  v = u32x2{k10[2], k10[3]}; *(u32x2*)(smem + (ma ^ 80)) = v;
          v = u32x2{k11[0], k11[1]}; *(u32x2*)(smem + (ma ^ 96)) = v;  v = u32x2{k11[2], k11[3]}; *(u32x2*)(smem + (ma ^ 112)) = v;
        }
        if (lane < 32) pdp[q * 160 + g * 32 + lane] = Z[0];
      }
    }
  } else {
    float* Af = (float*)smem;
    float* Mf = (float*)(smem + 40960);
    float* pf = (float*)(smem + 81920);
    u32* lst = (u32*)(smem + O_G);
    BAR_ALL();
    for (int i = tid; i < NPB * 256; i += 512) {
      const int n = i >> 8, f = i & 255;
      float a = 0.f;
      for (int k = 0; k < 256; ++k) a += bf2f(h[(node0 + n) * 256 + k]) * bf2f(we[k * 256 + f]);
      Af[i] = a * SCL; Mf[i] = 0.f;
    }
    if (tid < 160) pf[tid] = 0.f;
    for (int e0 = 0; e0 < E; e0 += 512) {
      if (tid == 0) gcnt[6] = 0;
      __syncthreads();
      const int e = e0 + tid;
      if (e < E) { const u32 dl = (u32)(dstg[e] - node0); if (dl < (u32)NPB) lst[atomicAdd(&gcnt[6], 1u)] = e; }
      __syncthreads();
      const int cnt = gcnt[6];
      for (int i = w; i < cnt; i += 8) {
        const int ee = lst[i], s = srcg[ee], dl = dstg[ee] - node0;
        const u32x2 pd = *(const u32x2*)(smem + O_POS + dl * 8), psv = *(const u32x2*)(ws + WS_POS4 + (size_t)s * 8);
        const float fx = bf2f(f2bf(bf2f(pd[0] & 0xffff) - bf2f(psv[0] & 0xffff))), fy = bf2f(f2bf(bf2f(pd[0] >> 16) - bf2f(psv[0] >> 16)));
        const float fz = bf2f(f2bf(bf2f(pd[1] & 0xffff) - bf2f(psv[1] & 0xffff)));
        const float d2 = bf2f(f2bf(bf2f(f2bf(fx * fx)) + bf2f(f2bf(fy * fy)) + bf2f(f2bf(fz * fz))));
        float cs = 0.f;
        for (int c = 0; c < 4; ++c) {
          const int f = lane + 64 * c;
          float y = d2 * bf2f(we[512 * 256 + f]);
          for (int k = 0; k < 16; ++k) y += bf2f(ea[ee * 16 + k]) * bf2f(we[(513 + k) * 256 + f]);
          y = y * SCL + Af[dl * 256 + f] + bf2f(((const u16*)(ws + WS_B))[s * 256 + f]);
          const float m = bf2f(f2bf(y * __builtin_amdgcn_rcpf(1.f + __builtin_amdgcn_exp2f(y)) * ISCL));
          atomicAdd(&Mf[dl * 256 + f], m);
          cs += m * bf2f(wxg[f]);
        }
        for (int o = 32; o; o >>= 1) cs += __shfl_xor(cs, o);
        if (lane < 3) atomicAdd(&pf[dl * 4 + lane], cs * (lane == 0 ? fx : lane == 1 ? fy : fz));
      }
      __syncthreads();
    }
    for (int i = tid; i < NPB * 256; i += 512) {
      const int n = i >> 8, f = i & 255;
      *(u16*)(smem + O_M + n * 512 + ((f >> 7) << 8) + 16 * (((f >> 3) & 15) ^ (n & 15)) + 2 * (f & 7)) = (u16)f2bf(Mf[i]);
    }
    float pv = 0.f;
    if (tid < 160 && (tid & 31) >= 8) pv = pf[((tid >> 5) * 8 + (tid & 7)) * 4 + ((tid >> 3) & 3) - 1];
    __syncthreads();
    if (tid < 160) { pdp[tid] = pv; pdp[160 + tid] = 0.f; pdp[320 + tid] = 0.f; pdp[480 + tid] = 0.f; }
  }
  STAMP(7);
  {
    const bf16x8* wp = (const bf16x8*)(ws + WS_WHF) + w * 2048 + lane;
    bf16x8 wf[16];
#pragma unroll
    for (int s = 0; s < 16; ++s) wf[s] = wp[s * 64];
    const u32 ba = r31 * 512 + 16 * (hh ^ (r31 & 15));
    const int n8 = lane & 7, u = (lane >> 3) & 1, g4 = lane >> 4;
    const bool act16 = (g4 & 1) == u;
    const u32 bc = (32 + n8) * 512 + 16 * ((g4 >> 1) ^ n8);
    const u32 bch = act16 ? O_H + bc : O_Z, bcm = act16 ? O_M + bc : O_Z;
    f32x16 c32 = {}; f32x4 c16 = {};
#pragma unroll
    for (int s = 0; s < 16; ++s) {
      c32 = MFMA32(wf[s], ld128(smem, O_H + (ba ^ (32 * (s & 7))) + 256 * (s >> 3)), c32);
      c16 = MFMA16(wf[s], ld128(smem, (bch ^ (32 * (s & 7))) + 256 * (s >> 3)), c16);
    }
#pragma unroll
    for (int s = 0; s < 16; ++s) wf[s] = wp[(16 + s) * 64];
    STAMP(8);
    BAR_LGKM();
    STAMP(9);
#pragma unroll
    for (int s = 0; s < 16; ++s) {
      c32 = MFMA32(wf[s], ld128(smem, O_M + (ba ^ (32 * (s & 7))) + 256 * (s >> 3)), c32);
      c16 = MFMA16(wf[s], ld128(smem, (bcm ^ (32 * (s & 7))) + 256 * (s >> 3)), c16);
    }
    auto silu = [](float z) { return z * __builtin_amdgcn_rcpf(1.f + __builtin_amdgcn_exp2f(z * SCL)); };
    {
      const u32 ch = 4 * w + 2 * hh;
      const u32 ha = O_H + r31 * 512 + (ch >> 4) * 256 + 16 * ((ch & 15) ^ (r31 & 15));
      u32x4* o = (u32x4*)(out + (size_t)(node0 + r31) * 256 + 32 * w + 16 * hh);
#pragma unroll
      for (int j = 0; j < 2; ++j) {
        const u32x4 hv = *(const u32x4*)(smem + (ha ^ (16 * j)));
        u32x4 v;
#pragma unroll
        for (int i = 0; i < 4; ++i)
          v[i] = pk2(bf2f(hv[i] & 0xffff) + silu(c32[8 * j + 2 * i]), bf2f(hv[i] >> 16) + silu(c32[8 * j + 2 * i + 1]));
        o[j] = v;
      }
    }
    {
      const u32 f = 32 * w + 16 * (g4 & 1) + 4 * (2 * u + (g4 >> 1)), ch = f >> 3;
      const u32x2 hv = *(const u32x2*)(smem + O_H + (32 + n8) * 512 + (ch >> 4) * 256 + 16 * ((ch & 15) ^ n8) + 2 * (f & 7));
      u32x2 v = {pk2(bf2f(hv[0] & 0xffff) + silu(c16[0]), bf2f(hv[0] >> 16) + silu(c16[1])),
                 pk2(bf2f(hv[1] & 0xffff) + silu(c16[2]), bf2f(hv[1] >> 16) + silu(c16[3]))};
      *(u32x2*)(out + (size_t)(node0 + 32 + n8) * 256 + f) = v;
    }
    if (tid < 3 * NPB) {
      const int n = tid / 3, c = tid - 3 * n, i = (n >> 3) * 32 + 8 * (c + 1) + (n & 7);
      const float d = (pdp[i] + pdp[160 + i]) + (pdp[320 + i] + pdp[480 + i]);
      const u32 pv = ((const u16*)(smem + O_POS))[n * 4 + c];
      out[(size_t)N * 256 + node0 * 3 + tid] = (u16)f2bf(bf2f(pv) + d);
    }
  }
  STAMP(10);
  asm volatile("s_waitcnt vmcnt(0)" ::: "memory");
  STAMP_FLUSH(1);
}

extern "C" void kernel_launch(void* const* d_in, const int* in_sizes, int n_in, void* d_out, int out_size, void* d_ws, size_t ws_size,
                              hipStream_t stream) {
  const u16* h = (const u16*)d_in[0];
  const u16* pos = (const u16*)d_in[1];
  const u16* ea = (const u16*)d_in[2];
  const int* src = (const int*)d_in[3];
  const int* dst = (const int*)d_in[4];
  const u16* we = (const u16*)d_in[5];
  const u16* wx = (const u16*)d_in[6];
  const u16* wh = (const u16*)d_in[7];
  k1<<<253, 512, 0, stream>>>(h, pos, src, dst, we, wx, wh, (char*)d_ws);
  k2<<<NB, 512, 0, stream>>>(h, ea, src, dst, we, wx, (const char*)d_ws, (u16*)d_out);
}
```

```cpp
#include <hip/hip_runtime.h>
#include <stdint.h>

typedef unsigned short u16;
typedef unsigned int u32;
typedef short bf16x8 __attribute__((ext_vector_type(8)));
typedef short s16x4 __attribute__((ext_vector_type(4)));
typedef float f32x16 __attribute__((ext_vector_type(16)));
typedef float f32x4 __attribute__((ext_vector_type(4)));
typedef float f32x2 __attribute__((ext_vector_type(2)));
typedef __bf16 bf2 __attribute__((ext_vector_type(2)));
typedef u32 u32x4 __attribute__((ext_vector_type(4)));
typedef u32 u32x2 __attribute__((ext_vector_type(2)));
typedef __attribute__((address_space(3))) s16x4* lds_s16x4_p;
typedef __attribute__((address_space(3))) char* lds_char_p;
#define DI __device__ __forceinline__
#define MFMA32(a, b, c) __builtin_amdgcn_mfma_f32_32x32x16_bf16((a), (b), (c), 0, 0, 0)
#define MFMA16(a, b, c) __builtin_amdgcn_mfma_f32_16x16x32_bf16((a), (b), (c), 0, 0, 0)
#define SB() __builtin_amdgcn_sched_barrier(0)

constexpr int N = 10000, E = 160000;
constexpr int NPB = 40, NB = 256;
constexpr int NCH = 64, CHE = 2500;
constexpr int TCAP = 26;
constexpr float SCL = -1.4426950408889634f;
constexpr float ISCL = -0.6931471805599453f;
constexpr u32 OOB = 0x40000000u;
constexpr int G0 = 3;
constexpr int PSLEEP = 4, WD = 10;

constexpr size_t WS_B = 4096;
constexpr size_t WS_CELL = 5124096;
constexpr size_t WS_W1F = WS_CELL + (size_t)NB * NCH * 128;
constexpr size_t WS_WHF = WS_W1F + 131072;
constexpr size_t WS_W4F = WS_WHF + 262144;
constexpr size_t WS_WDP = WS_W4F + 8192;
constexpr size_t WS_WXF = WS_WDP + 1024;
constexpr size_t WS_POS4 = WS_WXF + 512;
constexpr size_t WS_EA = WS_POS4 + 80384;
constexpr size_t WS_CX = WS_EA + 5120000;
constexpr size_t WS_FL = WS_CX + NB * 4096;

__host__ __device__ constexpr int bucket_base(int b) { return 40 * b < ((145 * b) >> 2) + 720 ? 40 * b : ((145 * b) >> 2) + 720; }
DI float bf2f(u32 x) { return __uint_as_float(x << 16); }
DI u32 pk2(float a, float b) { f32x2 v = {a, b}; return __builtin_bit_cast(u32, __builtin_convertvector(v, bf2)); }
DI u32 f2bf(float a) { return pk2(a, 0.f) & 0xffffu; }
DI u32 lds_addr(const void* p) { return (u32)(uintptr_t)(lds_char_p)p; }
DI __amdgpu_buffer_rsrc_t mk_rsrc(const void* p, u32 bytes) { return __builtin_amdgcn_make_buffer_rsrc((void*)p, (short)0, (int)bytes, 0x00020000); }
DI void dma16(u32 m0v, u32 voff, __amdgpu_buffer_rsrc_t r, u32 soff) {
  asm volatile("s_nop 4\n\ts_mov_b32 m0, %0\n\ts_nop 0\n\tbuffer_load_dwordx4 %1, %2, %3 offen lds" ::"s"(m0v), "v"(voff), "s"(r), "s"(soff) : "memory");
}
DI void dma16f(u32 m0v, u32 voff, __amdgpu_buffer_rsrc_t r, u32 soff) {
  asm volatile("s_mov_b32 m0, %0\n\ts_nop 0\n\tbuffer_load_dwordx4 %1, %2, %3 offen lds" ::"s"(m0v), "v"(voff), "s"(r), "s"(soff) : "memory");
}
DI void dma16n(u32 m0v, u32 voff, __amdgpu_buffer_rsrc_t r, u32 soff) {
  asm volatile("s_mov_b32 m0, %0\n\ts_nop 0\n\tbuffer_load_dwordx4 %1, %2, %3 offen nt lds" ::"s"(m0v), "v"(voff), "s"(r), "s"(soff) : "memory");
}
#define BAR_LGKM() asm volatile("s_waitcnt lgkmcnt(0)\n\ts_barrier" ::: "memory")
#define BAR_ALL() asm volatile("s_waitcnt vmcnt(0) lgkmcnt(0)\n\ts_barrier" ::: "memory")
DI bf16x8 ld128(const char* smem, u32 off) { return *(const bf16x8*)(smem + off); }
DI bf16x8 tr2(const char* smem, u32 o0, u32 o1) {
  s16x4 a = __builtin_amdgcn_ds_read_tr16_b64_v4i16((lds_s16x4_p)(lds_char_p)(smem + o0));
  s16x4 b = __builtin_amdgcn_ds_read_tr16_b64_v4i16((lds_s16x4_p)(lds_char_p)(smem + o1));
  return __builtin_shufflevector(a, b, 0, 1, 2, 3, 4, 5, 6, 7);
}
DI bf16x8 asbf(u32x4 v) { return __builtin_bit_cast(bf16x8, v); }

DI void lds_signal(u32* c, int) {
  asm volatile("s_mov_b64 exec, 1\n\tds_add_u32 %0, %1\n\ts_mov_b64 exec, -1" :: "v"(lds_addr(c)), "v"(1u) : "memory");
}
template <int SLEEP>
DI void lds_wait(u32* c, u32 target) {
  u32 t, s, n = 1u << 22;
  asm volatile("1:\n\tds_read_b32 %0, %3\n\ts_waitcnt lgkmcnt(0)\n\tv_readfirstlane_b32 %1, %0\n\ts_cmp_ge_u32 %1, %4\n\ts_cbranch_scc1 2f\n\t"
               "s_sleep %5\n\ts_sub_u32 %2, %2, 1\n\ts_cbranch_scc0 1b\n2:"
               : "=&v"(t), "=&s"(s), "+&s"(n) : "v"(lds_addr(c)), "s"(target), "i"(SLEEP) : "scc", "memory");
}
DI void dma16c(u32 m0v, u32 voff, __amdgpu_buffer_rsrc_t r) {
  asm volatile("s_mov_b32 m0, %0\n\ts_nop 0\n\tbuffer_load_dwordx4 %1, %2, 0 offen sc1 lds" ::"s"(m0v), "v"(voff), "s"(r) : "memory");
}
template <int ARR = 8>
DI void publish(u32* c, __amdgpu_buffer_rsrc_t rF, u32 slot, u32x2 tag, int lane) {
  u32 old = 0;
  if (lane == 0) old = __hip_atomic_fetch_add(c, 1u, __ATOMIC_RELAXED, __HIP_MEMORY_SCOPE_WORKGROUP);
  if (__builtin_amdgcn_readfirstlane(old) == ARR - 1 && lane == 0) __builtin_amdgcn_raw_buffer_store_b64(tag, rF, slot * 128, 0, 16);
}
template <int SLEEP>
DI void poll(__amdgpu_buffer_rsrc_t rF, u32 fo, u32x2 tag, u32x2& f) {
  for (int it = 1 << 18; it; --it) {
    if (__builtin_amdgcn_ballot_w64(fo != OOB && (f[0] != tag[0] || f[1] != tag[1])) == 0) break;
    if (SLEEP) __builtin_amdgcn_s_sleep(SLEEP);
    asm volatile("buffer_load_dwordx2 %0, %1, %2, 0 offen sc1\n\ts_waitcnt vmcnt(0)" : "=v"(f) : "v"(fo), "s"(rF) : "memory");
  }
}
constexpr u32 TS = 3264;
constexpr u32 T_A1 = 2048, T_D2 = 3072, T_SRC = 3136;
constexpr u32 O_EL = TCAP * TS;
constexpr u32 O_PD = O_EL;
constexpr u32 O_G = 88320;
constexpr u32 O_M = O_G + 32768;
constexpr u32 O_H = O_M + 20480;
constexpr u32 O_Z = O_H + 20480;
constexpr u32 O_CNT = O_Z + 512;
constexpr u32 O_POS = O_CNT + 128;
constexpr u32 O_WX = O_POS + 320;
constexpr u32 O_EP = O_WX + 512;
constexpr u32 LDS2 = O_EP + 128;

#define EDGE_ASM \
  "v_xor_b32 v244, 32, v243\n" \
  "v_xor_b32 v245, 64, v243\n" \
  "v_xor_b32 v246, 0x60, v243\n" \
  "ds_read_b128 v[160:163], v240 offset:2048\n" \
  "ds_read_b64_tr_b16 v[164:165], v241\n" \
  "ds_read_b64_tr_b16 v[166:167], v241 offset:64\n" \
  "ds_read_b128 v[144:147], v243\n" \
  "ds_read_b128 v[148:151], v244\n" \
  "ds_read_b128 v[152:155], v245\n" \
  "ds_read_b128 v[156:159], v246\n" \
  "ds_read_b128 v[176:179], v242 offset:3264\n" \
  "ds_read_b128 v[196:199], v251\n" \
  "ds_read_b128 v[200:203], v252\n" \
  "ds_read_b128 v[212:215], v28\n" \
  "ds_read_b128 v[216:219], v28 offset:32\n" \
  "ds_read_b128 v[220:223], v28 offset:64\n" \
  "ds_read_b128 v[224:227], v28 offset:96\n" \
  "v_xor_b32 v248, 64, v247\n" \
  "v_subrev_u32 v249, 0x800, v247\n" \
  "v_subrev_u32 v248, 0x400, v248\n" \
  "v_subrev_u32 v250, 0x800, v248\n" \
  "v_mov_b64 v[128:129], 0\n" \
  "v_mov_b64 v[130:131], 0\n" \
  "v_mov_b64 v[132:133], 0\n" \
  "v_mov_b64 v[134:135], 0\n" \
  "v_mov_b64 v[136:137], 0\n" \
  "v_mov_b64 v[138:139], 0\n" \
  "v_mov_b64 v[140:141], 0\n" \
  "v_mov_b64 v[142:143], 0\n" \
  "v_mov_b64 v[236:237], 0\n" \
  "v_mov_b64 v[238:239], 0\n" \
  "v_mov_b32 v31, 1\n" \
  "s_mov_b32 s76, %[once]\n" \
  "s_mov_b32 s77, 0\n" \
  "s_mov_b32 s68, %[nseq]\n" \
  "s_mov_b32 s69, %[nseqh]\n" \
  "s_mov_b32 vcc_lo, 0\n" \
  "s_mov_b32 vcc_hi, -1\n" \
  "s_mov_b64 s[60:61], 0\n" \
  "s_mov_b64 s[62:63], 0\n" \
  "s_mov_b64 s[64:65], 0\n" \
  "s_waitcnt lgkmcnt(5)\n" \
  "v_mfma_f32_32x32x16_bf16 v[32:47], v[160:163], v[180:183], 0\n" \
  "v_cndmask_b32 v188, v26, v196, vcc\n" \
  "v_cndmask_b32 v189, 0, v197, vcc\n" \
  "v_cndmask_b32 v190, 0, v198, vcc\n" \
  "v_cndmask_b32 v191, 0, v199, vcc\n" \
  "v_add_u32 v176, v176, v247\n" \
  "v_add_u32 v177, v177, v248\n" \
  "buffer_load_dwordx4 v176, %[rb], %[gso] offen sc1 lds\n" \
  "v_mfma_f32_32x32x16_bf16 v[32:47], v[164:167], v[188:191], v[32:47]\n" \
  "v_add_u32 v178, v178, v249\n" \
  "buffer_load_dwordx4 v177, %[rb], %[gso] offen offset:1024 sc1 lds\n" \
  "v_add_u32 v179, v179, v250\n" \
  "buffer_load_dwordx4 v178, %[rb], %[gso] offen offset:2048 sc1 lds\n" \
  "buffer_load_dwordx4 v179, %[rb], %[gso] offen offset:3072 sc1 lds\n" \
  "v_mfma_f32_32x32x16_bf16 v[32:47], v[144:147], v[204:207], v[32:47]\n" \
  "s_waitcnt lgkmcnt(0)\n" \
  "s_nop 3\n" \
  "v_mfma_f32_32x32x16_bf16 v[32:47], v[148:151], v[208:211], v[32:47]\n" \
  "10:\n" \
  "s_and_b32 %[tmp], s68, 0x3ff\n" \
  "s_lshr_b64 s[68:69], s[68:69], 10\n" \
  "s_lshr_b32 s67, %[tmp], 5\n" \
  "s_lshl_b32 s66, 1, s67\n" \
  "v_cndmask_b32 v192, v27, v200, vcc\n" \
  "v_cndmask_b32 v193, 0, v201, vcc\n" \
  "v_cndmask_b32 v194, 0, v202, vcc\n" \
  "v_cndmask_b32 v195, 0, v203, vcc\n" \
  "s_cmp_lg_u32 s66, 1\n" \
  "s_cbranch_scc1 55f\n" \
  "s_cmp_eq_u32 %[chk], 0\n" \
  "s_cbranch_scc1 54f\n" \
  "53:\n" \
  "ds_read_b32 v28, v30 offset:24\n" \
  "s_waitcnt lgkmcnt(0)\n" \
  "v_readfirstlane_b32 s67, v28\n" \
  "s_cmp_ge_u32 s67, 4\n" \
  "s_cselect_b32 %[chk], 1, %[chk]\n" \
  "s_sub_u32 %[chk], %[chk], 1\n" \
  "s_cmp_lg_u32 %[chk], 0\n" \
  "s_cbranch_scc1 53b\n" \
  "54:\n" \
  "55:\n" \
  "s_bitcmp1_b32 %[tmp], 4\n" \
  "s_cbranch_scc0 12f\n" \
  "s_bitcmp1_b32 %[tmp], 3\n" \
  "s_cbranch_scc0 13f\n" \
  "s_lshr_b32 s66, s66, 1\n" \
  "v_mfma_f32_32x32x16_bf16 v[48:63], v[160:163], v[184:187], 0\n" \
  "v_add_u32 v251, 0x1000, v251\n" \
  "v_add_u32 v252, 0x1000, v252\n" \
  "v_xor_b32 v251, 0x80, v251\n" \
  "v_xor_b32 v252, 0x80, v252\n" \
  "ds_read_b128 v[196:199], v251\n" \
  "ds_read_b128 v[200:203], v252\n" \
  "v_mfma_f32_32x32x16_bf16 v[48:63], v[164:167], v[192:195], v[48:63]\n" \
  "v_cvt_pk_bf16_f32 v228, v64, v65\n" \
  "v_cvt_pk_bf16_f32 v229, v66, v67\n" \
  "v_cvt_pk_bf16_f32 v230, v68, v69\n" \
  "v_cvt_pk_bf16_f32 v231, v70, v71\n" \
  "v_cvt_pk_bf16_f32 v232, v72, v73\n" \
  "v_cvt_pk_bf16_f32 v233, v74, v75\n" \
  "v_cvt_pk_bf16_f32 v234, v76, v77\n" \
  "v_cvt_pk_bf16_f32 v235, v78, v79\n" \
  "v_mfma_f32_32x32x16_bf16 v[48:63], v[152:155], v[204:207], v[48:63]\n" \
  "v_xor_b32 v28, 16, v253\n" \
  "v_xor_b32 v29, 32, v253\n" \
  "v_xor_b32 v255, 48, v253\n" \
  "s_mov_b64 exec, s[60:61]\n" \
  "ds_write_b64 v253, v[228:229]\n" \
  "ds_write_b64 v28, v[230:231]\n" \
  "ds_write_b64 v29, v[232:233]\n" \
  "ds_write_b64 v255, v[234:235]\n" \
  "s_mov_b64 exec, -1\n" \
  "v_mfma_f32_32x32x16_bf16 v[48:63], v[156:159], v[208:211], v[48:63]\n" \
  "v_exp_f32 v112, v32\n" \
  "v_exp_f32 v113, v33\n" \
  "v_exp_f32 v114, v34\n" \
  "v_exp_f32 v115, v35\n" \
  "v_mfma_f32_32x32x16_bf16 v[96:111], v[212:215], v[228:231], 0\n" \
  "v_exp_f32 v116, v36\n" \
  "v_exp_f32 v117, v37\n" \
  "v_exp_f32 v118, v38\n" \
  "v_exp_f32 v119, v39\n" \
  "v_mfma_f32_32x32x16_bf16 v[96:111], v[216:219], v[232:235], v[96:111]\n" \
  "v_exp_f32 v120, v40\n" \
  "v_exp_f32 v121, v41\n" \
  "v_exp_f32 v122, v42\n" \
  "v_exp_f32 v123, v43\n" \
  "v_exp_f32 v124, v44\n" \
  "v_exp_f32 v125, v45\n" \
  "v_exp_f32 v126, v46\n" \
  "v_exp_f32 v127, v47\n" \
  "v_pk_add_f32 v[112:113], v[112:113], 1.0 op_sel_hi:[1,0]\n" \
  "v_pk_add_f32 v[114:115], v[114:115], 1.0 op_sel_hi:[1,0]\n" \
  "v_pk_add_f32 v[116:117], v[116:117], 1.0 op_sel_hi:[1,0]\n" \
  "v_pk_add_f32 v[118:119], v[118:119], 1.0 op_sel_hi:[1,0]\n" \
  "v_pk_add_f32 v[120:121], v[120:121], 1.0 op_sel_hi:[1,0]\n" \
  "v_pk_add_f32 v[122:123], v[122:123], 1.0 op_sel_hi:[1,0]\n" \
  "v_pk_add_f32 v[124:125], v[124:125], 1.0 op_sel_hi:[1,0]\n" \
  "v_pk_add_f32 v[126:127], v[126:127], 1.0 op_sel_hi:[1,0]\n" \
  "v_rcp_f32 v112, v112\n" \
  "v_rcp_f32 v113, v113\n" \
  "v_mfma_f32_32x32x16_bf16 v[80:95], v[136:139], v[168:171], v[80:95]\n" \
  "v_rcp_f32 v114, v114\n" \
  "v_rcp_f32 v115, v115\n" \
  "v_rcp_f32 v116, v116\n" \
  "v_rcp_f32 v117, v117\n" \
  "v_mfma_f32_32x32x16_bf16 v[80:95], v[140:143], v[172:175], v[80:95]\n" \
  "v_rcp_f32 v118, v118\n" \
  "v_rcp_f32 v119, v119\n" \
  "s_waitcnt vmcnt(0) lgkmcnt(0)\n" \
  "s_mov_b64 exec, s[76:77]\n" \
  "ds_add_u32 v30, v31 offset:24\n" \
  "s_mov_b64 exec, -1\n" \
  "ds_read_b128 v[160:163], v240 offset:5312\n" \
  "ds_read_b64_tr_b16 v[164:165], v241 offset:3264\n" \
  "ds_read_b64_tr_b16 v[166:167], v241 offset:3328\n" \
  "ds_read_b128 v[144:147], v243\n" \
  "ds_read_b128 v[148:151], v244\n" \
  "ds_read_b128 v[152:155], v245\n" \
  "ds_read_b128 v[156:159], v246\n" \
  "ds_read_b128 v[176:179], v242 offset:6528\n" \
  "ds_read_b128 v[168:171], v240\n" \
  "ds_read_b128 v[172:175], v240 offset:1024\n" \
  "v_rcp_f32 v120, v120\n" \
  "v_rcp_f32 v121, v121\n" \
  "v_rcp_f32 v122, v122\n" \
  "v_rcp_f32 v123, v123\n" \
  "v_rcp_f32 v124, v124\n" \
  "v_rcp_f32 v125, v125\n" \
  "v_rcp_f32 v126, v126\n" \
  "v_rcp_f32 v127, v127\n" \
  "v_pk_mul_f32 v[112:113], v[112:113], v[32:33]\n" \
  "v_pk_mul_f32 v[114:115], v[114:115], v[34:35]\n" \
  "v_pk_mul_f32 v[116:117], v[116:117], v[36:37]\n" \
  "v_pk_mul_f32 v[118:119], v[118:119], v[38:39]\n" \
  "v_pk_mul_f32 v[120:121], v[120:121], v[40:41]\n" \
  "v_pk_mul_f32 v[122:123], v[122:123], v[42:43]\n" \
  "v_pk_mul_f32 v[124:125], v[124:125], v[44:45]\n" \
  "v_pk_mul_f32 v[126:127], v[126:127], v[46:47]\n" \
  "v_cvt_pk_bf16_f32 v128, v112, v113\n" \
  "v_cvt_pk_bf16_f32 v129, v114, v115\n" \
  "v_cvt_pk_bf16_f32 v130, v116, v117\n" \
  "v_cvt_pk_bf16_f32 v131, v118, v119\n" \
  "v_cvt_pk_bf16_f32 v132, v120, v121\n" \
  "v_cvt_pk_bf16_f32 v133, v122, v123\n" \
  "v_cvt_pk_bf16_f32 v134, v124, v125\n" \
  "v_cvt_pk_bf16_f32 v135, v126, v127\n" \
  "s_waitcnt lgkmcnt(0)\n" \
  "s_cbranch_scc1 21f\n" \
  "v_cndmask_b32 v188, v26, v196, vcc\n" \
  "v_cndmask_b32 v189, 0, v197, vcc\n" \
  "v_cndmask_b32 v190, 0, v198, vcc\n" \
  "v_cndmask_b32 v191, 0, v199, vcc\n" \
  "21:\n" \
  "v_mfma_f32_32x32x16_bf16 v[32:47], v[160:163], v[180:183], 0\n" \
  "v_cvt_pk_bf16_f32 v228, v80, v81\n" \
  "v_cvt_pk_bf16_f32 v229, v82, v83\n" \
  "v_cvt_pk_bf16_f32 v230, v84, v85\n" \
  "v_cvt_pk_bf16_f32 v231, v86, v87\n" \
  "v_cvt_pk_bf16_f32 v232, v88, v89\n" \
  "v_cvt_pk_bf16_f32 v233, v90, v91\n" \
  "v_cvt_pk_bf16_f32 v234, v92, v93\n" \
  "v_cvt_pk_bf16_f32 v235, v94, v95\n" \
  "v_mfma_f32_32x32x16_bf16 v[32:47], v[164:167], v[188:191], v[32:47]\n" \
  "v_xor_b32 v253, 64, v253\n" \
  "v_xor_b32 v28, 16, v253\n" \
  "v_xor_b32 v29, 32, v253\n" \
  "v_xor_b32 v255, 48, v253\n" \
  "s_mov_b64 exec, s[60:61]\n" \
  "ds_write_b64 v253, v[228:229]\n" \
  "ds_write_b64 v28, v[230:231]\n" \
  "ds_write_b64 v29, v[232:233]\n" \
  "ds_write_b64 v255, v[234:235]\n" \
  "s_mov_b64 exec, -1\n" \
  "v_mfma_f32_32x32x16_bf16 v[32:47], v[144:147], v[204:207], v[32:47]\n" \
  "v_add_u32 v176, v176, v247\n" \
  "v_add_u32 v177, v177, v248\n" \
  "buffer_load_dwordx4 v176, %[rb], %[gso] offen sc1 lds\n" \
  "v_add_u32 v178, v178, v249\n" \
  "buffer_load_dwordx4 v177, %[rb], %[gso] offen offset:1024 sc1 lds\n" \
  "v_add_u32 v179, v179, v250\n" \
  "buffer_load_dwordx4 v178, %[rb], %[gso] offen offset:2048 sc1 lds\n" \
  "buffer_load_dwordx4 v179, %[rb], %[gso] offen offset:3072 sc1 lds\n" \
  "v_mfma_f32_32x32x16_bf16 v[32:47], v[148:151], v[208:211], v[32:47]\n" \
  "v_exp_f32 v112, v48\n" \
  "v_exp_f32 v113, v49\n" \
  "v_exp_f32 v114, v50\n" \
  "v_exp_f32 v115, v51\n" \
  "v_mfma_f32_32x32x16_bf16 v[96:111], v[220:223], v[228:231], v[96:111]\n" \
  "v_exp_f32 v116, v52\n" \
  "v_exp_f32 v117, v53\n" \
  "v_exp_f32 v118, v54\n" \
  "v_exp_f32 v119, v55\n" \
  "v_mfma_f32_32x32x16_bf16 v[96:111], v[224:227], v[232:235], v[96:111]\n" \
  "v_exp_f32 v120, v56\n" \
  "v_exp_f32 v121, v57\n" \
  "v_exp_f32 v122, v58\n" \
  "v_exp_f32 v123, v59\n" \
  "v_exp_f32 v124, v60\n" \
  "v_exp_f32 v125, v61\n" \
  "v_exp_f32 v126, v62\n" \
  "v_exp_f32 v127, v63\n" \
  "v_pk_add_f32 v[112:113], v[112:113], 1.0 op_sel_hi:[1,0]\n" \
  "v_pk_add_f32 v[114:115], v[114:115], 1.0 op_sel_hi:[1,0]\n" \
  "v_pk_add_f32 v[116:117], v[116:117], 1.0 op_sel_hi:[1,0]\n" \
  "v_pk_add_f32 v[118:119], v[118:119], 1.0 op_sel_hi:[1,0]\n" \
  "v_pk_add_f32 v[120:121], v[120:121], 1.0 op_sel_hi:[1,0]\n" \
  "v_pk_add_f32 v[122:123], v[122:123], 1.0 op_sel_hi:[1,0]\n" \
  "v_pk_add_f32 v[124:125], v[124:125], 1.0 op_sel_hi:[1,0]\n" \
  "v_pk_add_f32 v[126:127], v[126:127], 1.0 op_sel_hi:[1,0]\n" \
  "v_rcp_f32 v112, v112\n" \
  "v_rcp_f32 v113, v113\n" \
  "v_mfma_f32_32x32x16_bf16 v[64:79], v[128:131], v[168:171], 0\n" \
  "v_rcp_f32 v114, v114\n" \
  "v_rcp_f32 v115, v115\n" \
  "v_rcp_f32 v116, v116\n" \
  "v_rcp_f32 v117, v117\n" \
  "v_mfma_f32_32x32x16_bf16 v[64:79], v[132:135], v[172:175], v[64:79]\n" \
  "v_rcp_f32 v118, v118\n" \
  "v_rcp_f32 v119, v119\n" \
  "v_rcp_f32 v120, v120\n" \
  "v_rcp_f32 v121, v121\n" \
  "v_mfma_f32_32x32x16_bf16 v[80:95], v[236:239], v[236:239], 0\n" \
  "v_rcp_f32 v122, v122\n" \
  "v_rcp_f32 v123, v123\n" \
  "v_rcp_f32 v124, v124\n" \
  "v_rcp_f32 v125, v125\n" \
  "v_rcp_f32 v126, v126\n" \
  "v_rcp_f32 v127, v127\n" \
  "v_add_u32 v240, 0xcc0, v240\n" \
  "v_add_u32 v241, 0xcc0, v241\n" \
  "v_add_u32 v242, 0xcc0, v242\n" \
  "v_add_u32 v253, 0x1000, v253\n" \
  "v_pk_mul_f32 v[112:113], v[112:113], v[48:49]\n" \
  "v_pk_mul_f32 v[114:115], v[114:115], v[50:51]\n" \
  "v_pk_mul_f32 v[116:117], v[116:117], v[52:53]\n" \
  "v_pk_mul_f32 v[118:119], v[118:119], v[54:55]\n" \
  "v_pk_mul_f32 v[120:121], v[120:121], v[56:57]\n" \
  "v_pk_mul_f32 v[122:123], v[122:123], v[58:59]\n" \
  "v_pk_mul_f32 v[124:125], v[124:125], v[60:61]\n" \
  "v_pk_mul_f32 v[126:127], v[126:127], v[62:63]\n" \
  "v_xor_b32 v253, 0xc0, v253\n" \
  "v_cvt_pk_bf16_f32 v136, v112, v113\n" \
  "v_cvt_pk_bf16_f32 v137, v114, v115\n" \
  "v_cvt_pk_bf16_f32 v138, v116, v117\n" \
  "v_cvt_pk_bf16_f32 v139, v118, v119\n" \
  "v_cvt_pk_bf16_f32 v140, v120, v121\n" \
  "v_cvt_pk_bf16_f32 v141, v122, v123\n" \
  "v_cvt_pk_bf16_f32 v142, v124, v125\n" \
  "v_cvt_pk_bf16_f32 v143, v126, v127\n" \
  "v_mul_f32 v28, 0xbf317218, v96\n" \
  "s_mov_b64 exec, s[62:63]\n" \
  "ds_write_b32 v254, v28\n" \
  "s_mov_b64 exec, s[64:65]\n" \
  "ds_add_u32 v30, v31\n" \
  "s_mov_b64 exec, -1\n" \
  "v_add_u32 v254, 0x80, v254\n" \
  "s_cbranch_scc0 30f\n" \
  "11:\n" \
  "s_lshr_b32 s66, s66, 1\n" \
  "s_cbranch_scc1 52f\n" \
  "s_cmp_eq_u32 %[chk], 0\n" \
  "s_cbranch_scc1 51f\n" \
  "50:\n" \
  "ds_read_b32 v28, v30 offset:24\n" \
  "s_waitcnt lgkmcnt(0)\n" \
  "v_readfirstlane_b32 s67, v28\n" \
  "s_cmp_ge_u32 s67, 4\n" \
  "s_cselect_b32 %[chk], 1, %[chk]\n" \
  "s_sub_u32 %[chk], %[chk], 1\n" \
  "s_cmp_lg_u32 %[chk], 0\n" \
  "s_cbranch_scc1 50b\n" \
  "51:\n" \
  "s_cmp_lg_u32 0, 0\n" \
  "52:\n" \
  "v_mfma_f32_32x32x16_bf16 v[48:63], v[160:163], v[184:187], 0\n" \
  "v_exp_f32 v112, v32\n" \
  "v_exp_f32 v113, v33\n" \
  "v_exp_f32 v114, v34\n" \
  "v_exp_f32 v115, v35\n" \
  "v_mfma_f32_32x32x16_bf16 v[48:63], v[164:167], v[192:195], v[48:63]\n" \
  "v_exp_f32 v116, v36\n" \
  "v_exp_f32 v117, v37\n" \
  "v_exp_f32 v118, v38\n" \
  "v_exp_f32 v119, v39\n" \
  "v_mfma_f32_32x32x16_bf16 v[48:63], v[152:155], v[204:207], v[48:63]\n" \
  "v_exp_f32 v120, v40\n" \
  "v_exp_f32 v121, v41\n" \
  "v_exp_f32 v122, v42\n" \
  "v_exp_f32 v123, v43\n" \
  "v_mfma_f32_32x32x16_bf16 v[48:63], v[156:159], v[208:211], v[48:63]\n" \
  "v_exp_f32 v124, v44\n" \
  "v_exp_f32 v125, v45\n" \
  "v_exp_f32 v126, v46\n" \
  "v_exp_f32 v127, v47\n" \
  "v_pk_add_f32 v[112:113], v[112:113], 1.0 op_sel_hi:[1,0]\n" \
  "v_pk_add_f32 v[114:115], v[114:115], 1.0 op_sel_hi:[1,0]\n" \
  "v_pk_add_f32 v[116:117], v[116:117], 1.0 op_sel_hi:[1,0]\n" \
  "v_pk_add_f32 v[118:119], v[118:119], 1.0 op_sel_hi:[1,0]\n" \
  "v_pk_add_f32 v[120:121], v[120:121], 1.0 op_sel_hi:[1,0]\n" \
  "v_pk_add_f32 v[122:123], v[122:123], 1.0 op_sel_hi:[1,0]\n" \
  "v_pk_add_f32 v[124:125], v[124:125], 1.0 op_sel_hi:[1,0]\n" \
  "v_pk_add_f32 v[126:127], v[126:127], 1.0 op_sel_hi:[1,0]\n" \
  "v_rcp_f32 v112, v112\n" \
  "v_rcp_f32 v113, v113\n" \
  "v_mfma_f32_32x32x16_bf16 v[80:95], v[136:139], v[168:171], v[80:95]\n" \
  "v_rcp_f32 v114, v114\n" \
  "v_rcp_f32 v115, v115\n" \
  "v_rcp_f32 v116, v116\n" \
  "v_rcp_f32 v117, v117\n" \
  "v_mfma_f32_32x32x16_bf16 v[80:95], v[140:143], v[172:175], v[80:95]\n" \
  "v_rcp_f32 v118, v118\n" \
  "v_rcp_f32 v119, v119\n" \
  "s_waitcnt vmcnt(0) lgkmcnt(0)\n" \
  "ds_read_b128 v[160:163], v240 offset:5312\n" \
  "ds_read_b64_tr_b16 v[164:165], v241 offset:3264\n" \
  "ds_read_b64_tr_b16 v[166:167], v241 offset:3328\n" \
  "ds_read_b128 v[144:147], v243\n" \
  "ds_read_b128 v[148:151], v244\n" \
  "ds_read_b128 v[152:155], v245\n" \
  "ds_read_b128 v[156:159], v246\n" \
  "ds_read_b128 v[176:179], v242 offset:6528\n" \
  "ds_read_b128 v[168:171], v240\n" \
  "ds_read_b128 v[172:175], v240 offset:1024\n" \
  "v_rcp_f32 v120, v120\n" \
  "v_rcp_f32 v121, v121\n" \
  "v_rcp_f32 v122, v122\n" \
  "v_rcp_f32 v123, v123\n" \
  "v_rcp_f32 v124, v124\n" \
  "v_rcp_f32 v125, v125\n" \
  "v_rcp_f32 v126, v126\n" \
  "v_rcp_f32 v127, v127\n" \
  "v_pk_mul_f32 v[112:113], v[112:113], v[32:33]\n" \
  "v_pk_mul_f32 v[114:115], v[114:115], v[34:35]\n" \
  "v_pk_mul_f32 v[116:117], v[116:117], v[36:37]\n" \
  "v_pk_mul_f32 v[118:119], v[118:119], v[38:39]\n" \
  "v_pk_mul_f32 v[120:121], v[120:121], v[40:41]\n" \
  "v_pk_mul_f32 v[122:123], v[122:123], v[42:43]\n" \
  "v_pk_mul_f32 v[124:125], v[124:125], v[44:45]\n" \
  "v_pk_mul_f32 v[126:127], v[126:127], v[46:47]\n" \
  "v_cvt_pk_bf16_f32 v128, v112, v113\n" \
  "v_cvt_pk_bf16_f32 v129, v114, v115\n" \
  "v_cvt_pk_bf16_f32 v130, v116, v117\n" \
  "v_cvt_pk_bf16_f32 v131, v118, v119\n" \
  "v_cvt_pk_bf16_f32 v132, v120, v121\n" \
  "v_cvt_pk_bf16_f32 v133, v122, v123\n" \
  "v_cvt_pk_bf16_f32 v134, v124, v125\n" \
  "v_cvt_pk_bf16_f32 v135, v126, v127\n" \
  "s_cbranch_scc1 20f\n" \
  "v_cndmask_b32 v188, v26, v196, vcc\n" \
  "v_cndmask_b32 v189, 0, v197, vcc\n" \
  "v_cndmask_b32 v190, 0, v198, vcc\n" \
  "v_cndmask_b32 v191, 0, v199, vcc\n" \
  "20:\n" \
  "s_waitcnt lgkmcnt(2)\n" \
  "v_mfma_f32_32x32x16_bf16 v[32:47], v[160:163], v[180:183], 0\n" \
  "v_exp_f32 v112, v48\n" \
  "v_exp_f32 v113, v49\n" \
  "v_exp_f32 v114, v50\n" \
  "v_exp_f32 v115, v51\n" \
  "v_mfma_f32_32x32x16_bf16 v[32:47], v[164:167], v[188:191], v[32:47]\n" \
  "v_exp_f32 v116, v52\n" \
  "v_exp_f32 v117, v53\n" \
  "v_exp_f32 v118, v54\n" \
  "v_exp_f32 v119, v55\n" \
  "v_mfma_f32_32x32x16_bf16 v[32:47], v[144:147], v[204:207], v[32:47]\n" \
  "v_exp_f32 v120, v56\n" \
  "v_exp_f32 v121, v57\n" \
  "v_exp_f32 v122, v58\n" \
  "v_exp_f32 v123, v59\n" \
  "v_mfma_f32_32x32x16_bf16 v[32:47], v[148:151], v[208:211], v[32:47]\n" \
  "v_exp_f32 v124, v60\n" \
  "v_exp_f32 v125, v61\n" \
  "v_exp_f32 v126, v62\n" \
  "v_exp_f32 v127, v63\n" \
  "v_add_u32 v176, v176, v247\n" \
  "v_add_u32 v177, v177, v248\n" \
  "buffer_load_dwordx4 v176, %[rb], %[gso] offen sc1 lds\n" \
  "v_add_u32 v178, v178, v249\n" \
  "buffer_load_dwordx4 v177, %[rb], %[gso] offen offset:1024 sc1 lds\n" \
  "v_add_u32 v179, v179, v250\n" \
  "buffer_load_dwordx4 v178, %[rb], %[gso] offen offset:2048 sc1 lds\n" \
  "buffer_load_dwordx4 v179, %[rb], %[gso] offen offset:3072 sc1 lds\n" \
  "v_pk_add_f32 v[112:113], v[112:113], 1.0 op_sel_hi:[1,0]\n" \
  "v_pk_add_f32 v[114:115], v[114:115], 1.0 op_sel_hi:[1,0]\n" \
  "v_pk_add_f32 v[116:117], v[116:117], 1.0 op_sel_hi:[1,0]\n" \
  "v_pk_add_f32 v[118:119], v[118:119], 1.0 op_sel_hi:[1,0]\n" \
  "v_pk_add_f32 v[120:121], v[120:121], 1.0 op_sel_hi:[1,0]\n" \
  "v_pk_add_f32 v[122:123], v[122:123], 1.0 op_sel_hi:[1,0]\n" \
  "v_pk_add_f32 v[124:125], v[124:125], 1.0 op_sel_hi:[1,0]\n" \
  "v_pk_add_f32 v[126:127], v[126:127], 1.0 op_sel_hi:[1,0]\n" \
  "v_rcp_f32 v112, v112\n" \
  "v_rcp_f32 v113, v113\n" \
  "s_waitcnt lgkmcnt(0)\n" \
  "v_mfma_f32_32x32x16_bf16 v[64:79], v[128:131], v[168:171], v[64:79]\n" \
  "v_rcp_f32 v114, v114\n" \
  "v_rcp_f32 v115, v115\n" \
  "v_rcp_f32 v116, v116\n" \
  "v_rcp_f32 v117, v117\n" \
  "v_mfma_f32_32x32x16_bf16 v[64:79], v[132:135], v[172:175], v[64:79]\n" \
  "v_rcp_f32 v118, v118\n" \
  "v_rcp_f32 v119, v119\n" \
  "v_rcp_f32 v120, v120\n" \
  "v_rcp_f32 v121, v121\n" \
  "v_rcp_f32 v122, v122\n" \
  "v_rcp_f32 v123, v123\n" \
  "v_rcp_f32 v124, v124\n" \
  "v_rcp_f32 v125, v125\n" \
  "v_rcp_f32 v126, v126\n" \
  "v_rcp_f32 v127, v127\n" \
  "v_add_u32 v240, 0xcc0, v240\n" \
  "v_add_u32 v241, 0xcc0, v241\n" \
  "v_add_u32 v242, 0xcc0, v242\n" \
  "v_pk_mul_f32 v[112:113], v[112:113], v[48:49]\n" \
  "v_pk_mul_f32 v[114:115], v[114:115], v[50:51]\n" \
  "v_pk_mul_f32 v[116:117], v[116:117], v[52:53]\n" \
  "v_pk_mul_f32 v[118:119], v[118:119], v[54:55]\n" \
  "v_pk_mul_f32 v[120:121], v[120:121], v[56:57]\n" \
  "v_pk_mul_f32 v[122:123], v[122:123], v[58:59]\n" \
  "v_pk_mul_f32 v[124:125], v[124:125], v[60:61]\n" \
  "v_pk_mul_f32 v[126:127], v[126:127], v[62:63]\n" \
  "v_cvt_pk_bf16_f32 v136, v112, v113\n" \
  "v_cvt_pk_bf16_f32 v137, v114, v115\n" \
  "v_cvt_pk_bf16_f32 v138, v116, v117\n" \
  "v_cvt_pk_bf16_f32 v139, v118, v119\n" \
  "v_cvt_pk_bf16_f32 v140, v120, v121\n" \
  "v_cvt_pk_bf16_f32 v141, v122, v123\n" \
  "v_cvt_pk_bf16_f32 v142, v124, v125\n" \
  "v_cvt_pk_bf16_f32 v143, v126, v127\n" \
  "s_cbranch_scc1 11b\n" \
  "30:\n" \
  "s_mov_b32 s76, 0\n" \
  "s_mov_b32 s60, 0xff\n" \
  "s_mov_b32 s61, 0xff\n" \
  "s_mov_b32 s62, -1\n" \
  "s_sub_u32 %[ngr], %[ngr], 1\n" \
  "s_cmp_eq_u32 %[ngr], 1\n" \
  "s_cselect_b32 s64, %[sig], 0\n" \
  "s_cmp_lg_u32 %[ngr], 0\n" \
  "s_cbranch_scc1 10b\n" \
  "s_branch 40f\n" \
  "13:\n" \
  "s_lshr_b32 s66, s66, 1\n" \
  "v_mfma_f32_32x32x16_bf16 v[48:63], v[160:163], v[184:187], 0\n" \
  "v_add_u32 v251, 0x1000, v251\n" \
  "v_add_u32 v252, 0x1000, v252\n" \
  "v_xor_b32 v251, 0x80, v251\n" \
  "v_xor_b32 v252, 0x80, v252\n" \
  "ds_read_b128 v[196:199], v251\n" \
  "ds_read_b128 v[200:203], v252\n" \
  "v_mfma_f32_32x32x16_bf16 v[48:63], v[164:167], v[192:195], v[48:63]\n" \
  "v_cvt_pk_bf16_f32 v228, v64, v65\n" \
  "v_cvt_pk_bf16_f32 v229, v66, v67\n" \
  "v_cvt_pk_bf16_f32 v230, v68, v69\n" \
  "v_cvt_pk_bf16_f32 v231, v70, v71\n" \
  "v_cvt_pk_bf16_f32 v232, v72, v73\n" \
  "v_cvt_pk_bf16_f32 v233, v74, v75\n" \
  "v_cvt_pk_bf16_f32 v234, v76, v77\n" \
  "v_cvt_pk_bf16_f32 v235, v78, v79\n" \
  "v_mfma_f32_32x32x16_bf16 v[48:63], v[152:155], v[204:207], v[48:63]\n" \
  "v_xor_b32 v28, 16, v253\n" \
  "v_xor_b32 v29, 32, v253\n" \
  "v_xor_b32 v255, 48, v253\n" \
  "s_mov_b64 exec, s[60:61]\n" \
  "ds_write_b64 v253, v[228:229]\n" \
  "ds_write_b64 v28, v[230:231]\n" \
  "ds_write_b64 v29, v[232:233]\n" \
  "ds_write_b64 v255, v[234:235]\n" \
  "s_mov_b64 exec, -1\n" \
  "v_mfma_f32_32x32x16_bf16 v[48:63], v[156:159], v[208:211], v[48:63]\n" \
  "v_exp_f32 v116, v36\n" \
  "v_exp_f32 v117, v37\n" \
  "v_exp_f32 v118, v38\n" \
  "v_exp_f32 v119, v39\n" \
  "v_mfma_f32_32x32x16_bf16 v[96:111], v[212:215], v[228:231], 0\n" \
  "v_exp_f32 v120, v40\n" \
  "v_exp_f32 v121, v41\n" \
  "v_exp_f32 v122, v42\n" \
  "v_exp_f32 v123, v43\n" \
  "v_mfma_f32_32x32x16_bf16 v[96:111], v[216:219], v[232:235], v[96:111]\n" \
  "v_exp_f32 v124, v44\n" \
  "v_exp_f32 v125, v45\n" \
  "v_exp_f32 v126, v46\n" \
  "v_exp_f32 v127, v47\n" \
  "v_pk_add_f32 v[116:117], v[116:117], 1.0 op_sel_hi:[1,0]\n" \
  "v_pk_add_f32 v[118:119], v[118:119], 1.0 op_sel_hi:[1,0]\n" \
  "v_pk_add_f32 v[120:121], v[120:121], 1.0 op_sel_hi:[1,0]\n" \
  "v_pk_add_f32 v[122:123], v[122:123], 1.0 op_sel_hi:[1,0]\n" \
  "v_pk_add_f32 v[124:125], v[124:125], 1.0 op_sel_hi:[1,0]\n" \
  "v_pk_add_f32 v[126:127], v[126:127], 1.0 op_sel_hi:[1,0]\n" \
  "v_rcp_f32 v116, v116\n" \
  "v_rcp_f32 v117, v117\n" \
  "v_mfma_f32_32x32x16_bf16 v[80:95], v[136:139], v[168:171], v[80:95]\n" \
  "v_rcp_f32 v118, v118\n" \
  "v_rcp_f32 v119, v119\n" \
  "v_rcp_f32 v120, v120\n" \
  "v_rcp_f32 v121, v121\n" \
  "v_mfma_f32_32x32x16_bf16 v[80:95], v[140:143], v[172:175], v[80:95]\n" \
  "v_rcp_f32 v122, v122\n" \
  "v_rcp_f32 v123, v123\n" \
  "s_waitcnt vmcnt(0) lgkmcnt(0)\n" \
  "s_mov_b64 exec, s[76:77]\n" \
  "ds_add_u32 v30, v31 offset:24\n" \
  "s_mov_b64 exec, -1\n" \
  "ds_read_b128 v[160:163], v240 offset:5312\n" \
  "ds_read_b64_tr_b16 v[164:165], v241 offset:3264\n" \
  "ds_read_b64_tr_b16 v[166:167], v241 offset:3328\n" \
  "ds_read_b128 v[144:147], v243\n" \
  "ds_read_b128 v[148:151], v244\n" \
  "ds_read_b128 v[152:155], v245\n" \
  "ds_read_b128 v[156:159], v246\n" \
  "ds_read_b128 v[176:179], v242 offset:6528\n" \
  "ds_read_b128 v[168:171], v240\n" \
  "ds_read_b128 v[172:175], v240 offset:1024\n" \
  "v_rcp_f32 v124, v124\n" \
  "v_rcp_f32 v125, v125\n" \
  "v_rcp_f32 v126, v126\n" \
  "v_rcp_f32 v127, v127\n" \
  "v_pk_mul_f32 v[116:117], v[116:117], v[36:37]\n" \
  "v_pk_mul_f32 v[118:119], v[118:119], v[38:39]\n" \
  "v_pk_mul_f32 v[120:121], v[120:121], v[40:41]\n" \
  "v_pk_mul_f32 v[122:123], v[122:123], v[42:43]\n" \
  "v_pk_mul_f32 v[124:125], v[124:125], v[44:45]\n" \
  "v_pk_mul_f32 v[126:127], v[126:127], v[46:47]\n" \
  "v_cvt_pk_bf16_f32 v130, v116, v117\n" \
  "v_cvt_pk_bf16_f32 v131, v118, v119\n" \
  "v_cvt_pk_bf16_f32 v132, v120, v121\n" \
  "v_cvt_pk_bf16_f32 v133, v122, v123\n" \
  "v_cvt_pk_bf16_f32 v134, v124, v125\n" \
  "v_cvt_pk_bf16_f32 v135, v126, v127\n" \
  "s_waitcnt lgkmcnt(0)\n" \
  "s_cbranch_scc1 22f\n" \
  "v_cndmask_b32 v188, v26, v196, vcc\n" \
  "v_cndmask_b32 v189, 0, v197, vcc\n" \
  "v_cndmask_b32 v190, 0, v198, vcc\n" \
  "v_cndmask_b32 v191, 0, v199, vcc\n" \
  "22:\n" \
  "v_mfma_f32_32x32x16_bf16 v[32:47], v[160:163], v[180:183], 0\n" \
  "v_cvt_pk_bf16_f32 v228, v80, v81\n" \
  "v_cvt_pk_bf16_f32 v229, v82, v83\n" \
  "v_cvt_pk_bf16_f32 v230, v84, v85\n" \
  "v_cvt_pk_bf16_f32 v231, v86, v87\n" \
  "v_cvt_pk_bf16_f32 v232, v88, v89\n" \
  "v_cvt_pk_bf16_f32 v233, v90, v91\n" \
  "v_cvt_pk_bf16_f32 v234, v92, v93\n" \
  "v_cvt_pk_bf16_f32 v235, v94, v95\n" \
  "v_mfma_f32_32x32x16_bf16 v[32:47], v[164:167], v[188:191], v[32:47]\n" \
  "v_xor_b32 v253, 64, v253\n" \
  "v_xor_b32 v28, 16, v253\n" \
  "v_xor_b32 v29, 32, v253\n" \
  "v_xor_b32 v255, 48, v253\n" \
  "s_mov_b64 exec, s[60:61]\n" \
  "ds_write_b64 v253, v[228:229]\n" \
  "ds_write_b64 v28, v[230:231]\n" \
  "ds_write_b64 v29, v[232:233]\n" \
  "ds_write_b64 v255, v[234:235]\n" \
  "s_mov_b64 exec, -1\n" \
  "v_mfma_f32_32x32x16_bf16 v[32:47], v[144:147], v[204:207], v[32:47]\n" \
  "v_add_u32 v176, v176, v247\n" \
  "v_add_u32 v177, v177, v248\n" \
  "buffer_load_dwordx4 v176, %[rb], %[gso] offen sc1 lds\n" \
  "v_add_u32 v178, v178, v249\n" \
  "buffer_load_dwordx4 v177, %[rb], %[gso] offen offset:1024 sc1 lds\n" \
  "v_add_u32 v179, v179, v250\n" \
  "buffer_load_dwordx4 v178, %[rb], %[gso] offen offset:2048 sc1 lds\n" \
  "buffer_load_dwordx4 v179, %[rb], %[gso] offen offset:3072 sc1 lds\n" \
  "v_mfma_f32_32x32x16_bf16 v[32:47], v[148:151], v[208:211], v[32:47]\n" \
  "v_exp_f32 v116, v52\n" \
  "v_exp_f32 v117, v53\n" \
  "v_exp_f32 v118, v54\n" \
  "v_exp_f32 v119, v55\n" \
  "v_mfma_f32_32x32x16_bf16 v[96:111], v[220:223], v[228:231], v[96:111]\n" \
  "v_exp_f32 v120, v56\n" \
  "v_exp_f32 v121, v57\n" \
  "v_exp_f32 v122, v58\n" \
  "v_exp_f32 v123, v59\n" \
  "v_mfma_f32_32x32x16_bf16 v[96:111], v[224:227], v[232:235], v[96:111]\n" \
  "v_exp_f32 v124, v60\n" \
  "v_exp_f32 v125, v61\n" \
  "v_exp_f32 v126, v62\n" \
  "v_exp_f32 v127, v63\n" \
  "v_pk_add_f32 v[116:117], v[116:117], 1.0 op_sel_hi:[1,0]\n" \
  "v_pk_add_f32 v[118:119], v[118:119], 1.0 op_sel_hi:[1,0]\n" \
  "v_pk_add_f32 v[120:121], v[120:121], 1.0 op_sel_hi:[1,0]\n" \
  "v_pk_add_f32 v[122:123], v[122:123], 1.0 op_sel_hi:[1,0]\n" \
  "v_pk_add_f32 v[124:125], v[124:125], 1.0 op_sel_hi:[1,0]\n" \
  "v_pk_add_f32 v[126:127], v[126:127], 1.0 op_sel_hi:[1,0]\n" \
  "v_rcp_f32 v116, v116\n" \
  "v_rcp_f32 v117, v117\n" \
  "v_mfma_f32_32x32x16_bf16 v[64:79], v[128:131], v[168:171], 0\n" \
  "v_rcp_f32 v118, v118\n" \
  "v_rcp_f32 v119, v119\n" \
  "v_rcp_f32 v120, v120\n" \
  "v_rcp_f32 v121, v121\n" \
  "v_mfma_f32_32x32x16_bf16 v[64:79], v[132:135], v[172:175], v[64:79]\n" \
  "v_rcp_f32 v122, v122\n" \
  "v_rcp_f32 v123, v123\n" \
  "v_rcp_f32 v124, v124\n" \
  "v_rcp_f32 v125, v125\n" \
  "v_mfma_f32_32x32x16_bf16 v[80:95], v[236:239], v[236:239], 0\n" \
  "v_rcp_f32 v126, v126\n" \
  "v_rcp_f32 v127, v127\n" \
  "v_add_u32 v240, 0xcc0, v240\n" \
  "v_add_u32 v241, 0xcc0, v241\n" \
  "v_add_u32 v242, 0xcc0, v242\n" \
  "v_add_u32 v253, 0x1000, v253\n" \
  "v_pk_mul_f32 v[116:117], v[116:117], v[52:53]\n" \
  "v_pk_mul_f32 v[118:119], v[118:119], v[54:55]\n" \
  "v_pk_mul_f32 v[120:121], v[120:121], v[56:57]\n" \
  "v_pk_mul_f32 v[122:123], v[122:123], v[58:59]\n" \
  "v_pk_mul_f32 v[124:125], v[124:125], v[60:61]\n" \
  "v_pk_mul_f32 v[126:127], v[126:127], v[62:63]\n" \
  "v_xor_b32 v253, 0xc0, v253\n" \
  "v_cvt_pk_bf16_f32 v138, v116, v117\n" \
  "v_cvt_pk_bf16_f32 v139, v118, v119\n" \
  "v_cvt_pk_bf16_f32 v140, v120, v121\n" \
  "v_cvt_pk_bf16_f32 v141, v122, v123\n" \
  "v_cvt_pk_bf16_f32 v142, v124, v125\n" \
  "v_cvt_pk_bf16_f32 v143, v126, v127\n" \
  "v_mul_f32 v28, 0xbf317218, v96\n" \
  "s_mov_b64 exec, s[62:63]\n" \
  "ds_write_b32 v254, v28\n" \
  "s_mov_b64 exec, s[64:65]\n" \
  "ds_add_u32 v30, v31\n" \
  "s_mov_b64 exec, -1\n" \
  "v_add_u32 v254, 0x80, v254\n" \
  "s_cbranch_scc1 11b\n" \
  "s_branch 30b\n" \
  "12:\n" \
  "s_bitcmp1_b32 %[tmp], 3\n" \
  "s_cbranch_scc0 14f\n" \
  "s_lshr_b32 s66, s66, 1\n" \
  "v_mfma_f32_32x32x16_bf16 v[48:63], v[160:163], v[184:187], 0\n" \
  "v_add_u32 v251, 0x1000, v251\n" \
  "v_add_u32 v252, 0x1000, v252\n" \
  "v_xor_b32 v251, 0x80, v251\n" \
  "v_xor_b32 v252, 0x80, v252\n" \
  "ds_read_b128 v[196:199], v251\n" \
  "ds_read_b128 v[200:203], v252\n" \
  "v_mfma_f32_32x32x16_bf16 v[48:63], v[164:167], v[192:195], v[48:63]\n" \
  "v_cvt_pk_bf16_f32 v228, v64, v65\n" \
  "v_cvt_pk_bf16_f32 v229, v66, v67\n" \
  "v_cvt_pk_bf16_f32 v230, v68, v69\n" \
  "v_cvt_pk_bf16_f32 v231, v70, v71\n" \
  "v_cvt_pk_bf16_f32 v232, v72, v73\n" \
  "v_cvt_pk_bf16_f32 v233, v74, v75\n" \
  "v_cvt_pk_bf16_f32 v234, v76, v77\n" \
  "v_cvt_pk_bf16_f32 v235, v78, v79\n" \
  "v_mfma_f32_32x32x16_bf16 v[48:63], v[152:155], v[204:207], v[48:63]\n" \
  "v_xor_b32 v28, 16, v253\n" \
  "v_xor_b32 v29, 32, v253\n" \
  "v_xor_b32 v255, 48, v253\n" \
  "s_mov_b64 exec, s[60:61]\n" \
  "ds_write_b64 v253, v[228:229]\n" \
  "ds_write_b64 v28, v[230:231]\n" \
  "ds_write_b64 v29, v[232:233]\n" \
  "ds_write_b64 v255, v[234:235]\n" \
  "s_mov_b64 exec, -1\n" \
  "v_mfma_f32_32x32x16_bf16 v[48:63], v[156:159], v[208:211], v[48:63]\n" \
  "v_exp_f32 v120, v40\n" \
  "v_exp_f32 v121, v41\n" \
  "v_exp_f32 v122, v42\n" \
  "v_exp_f32 v123, v43\n" \
  "v_exp_f32 v124, v44\n" \
  "v_exp_f32 v125, v45\n" \
  "v_exp_f32 v126, v46\n" \
  "v_exp_f32 v127, v47\n" \
  "v_pk_add_f32 v[120:121], v[120:121], 1.0 op_sel_hi:[1,0]\n" \
  "v_pk_add_f32 v[122:123], v[122:123], 1.0 op_sel_hi:[1,0]\n" \
  "v_pk_add_f32 v[124:125], v[124:125], 1.0 op_sel_hi:[1,0]\n" \
  "v_pk_add_f32 v[126:127], v[126:127], 1.0 op_sel_hi:[1,0]\n" \
  "v_rcp_f32 v120, v120\n" \
  "v_rcp_f32 v121, v121\n" \
  "v_mfma_f32_32x32x16_bf16 v[80:95], v[136:139], v[168:171], v[80:95]\n" \
  "s_waitcnt vmcnt(0) lgkmcnt(0)\n" \
  "s_mov_b64 exec, s[76:77]\n" \
  "ds_add_u32 v30, v31 offset:24\n" \
  "s_mov_b64 exec, -1\n" \
  "ds_read_b128 v[160:163], v240 offset:5312\n" \
  "ds_read_b64_tr_b16 v[164:165], v241 offset:3264\n" \
  "ds_read_b64_tr_b16 v[166:167], v241 offset:3328\n" \
  "ds_read_b128 v[144:147], v243\n" \
  "ds_read_b128 v[148:151], v244\n" \
  "ds_read_b128 v[152:155], v245\n" \
  "ds_read_b128 v[156:159], v246\n" \
  "ds_read_b128 v[176:179], v242 offset:6528\n" \
  "v_mfma_f32_32x32x16_bf16 v[80:95], v[140:143], v[172:175], v[80:95]\n" \
  "ds_read_b128 v[168:171], v240\n" \
  "ds_read_b128 v[172:175], v240 offset:1024\n" \
  "v_rcp_f32 v122, v122\n" \
  "v_rcp_f32 v123, v123\n" \
  "v_mfma_f32_32x32x16_bf16 v[96:111], v[212:215], v[228:231], 0\n" \
  "v_rcp_f32 v124, v124\n" \
  "v_rcp_f32 v125, v125\n" \
  "v_rcp_f32 v126, v126\n" \
  "v_rcp_f32 v127, v127\n" \
  "v_mfma_f32_32x32x16_bf16 v[96:111], v[216:219], v[232:235], v[96:111]\n" \
  "v_pk_mul_f32 v[120:121], v[120:121], v[40:41]\n" \
  "v_pk_mul_f32 v[122:123], v[122:123], v[42:43]\n" \
  "v_pk_mul_f32 v[124:125], v[124:125], v[44:45]\n" \
  "v_pk_mul_f32 v[126:127], v[126:127], v[46:47]\n" \
  "v_cvt_pk_bf16_f32 v132, v120, v121\n" \
  "v_cvt_pk_bf16_f32 v133, v122, v123\n" \
  "v_cvt_pk_bf16_f32 v134, v124, v125\n" \
  "v_cvt_pk_bf16_f32 v135, v126, v127\n" \
  "s_waitcnt lgkmcnt(0)\n" \
  "s_cbranch_scc1 23f\n" \
  "v_cndmask_b32 v188, v26, v196, vcc\n" \
  "v_cndmask_b32 v189, 0, v197, vcc\n" \
  "v_cndmask_b32 v190, 0, v198, vcc\n" \
  "v_cndmask_b32 v191, 0, v199, vcc\n" \
  "23:\n" \
  "v_mfma_f32_32x32x16_bf16 v[32:47], v[160:163], v[180:183], 0\n" \
  "v_cvt_pk_bf16_f32 v228, v80, v81\n" \
  "v_cvt_pk_bf16_f32 v229, v82, v83\n" \
  "v_cvt_pk_bf16_f32 v230, v84, v85\n" \
  "v_cvt_pk_bf16_f32 v231, v86, v87\n" \
  "v_cvt_pk_bf16_f32 v232, v88, v89\n" \
  "v_cvt_pk_bf16_f32 v233, v90, v91\n" \
  "v_cvt_pk_bf16_f32 v234, v92, v93\n" \
  "v_cvt_pk_bf16_f32 v235, v94, v95\n" \
  "v_mfma_f32_32x32x16_bf16 v[32:47], v[164:167], v[188:191], v[32:47]\n" \
  "v_xor_b32 v253, 64, v253\n" \
  "v_xor_b32 v28, 16, v253\n" \
  "v_xor_b32 v29, 32, v253\n" \
  "v_xor_b32 v255, 48, v253\n" \
  "s_mov_b64 exec, s[60:61]\n" \
  "ds_write_b64 v253, v[228:229]\n" \
  "ds_write_b64 v28, v[230:231]\n" \
  "ds_write_b64 v29, v[232:233]\n" \
  "ds_write_b64 v255, v[234:235]\n" \
  "s_mov_b64 exec, -1\n" \
  "v_mfma_f32_32x32x16_bf16 v[32:47], v[144:147], v[204:207], v[32:47]\n" \
  "v_add_u32 v176, v176, v247\n" \
  "v_add_u32 v177, v177, v248\n" \
  "buffer_load_dwordx4 v176, %[rb], %[gso] offen sc1 lds\n" \
  "v_add_u32 v178, v178, v249\n" \
  "buffer_load_dwordx4 v177, %[rb], %[gso] offen offset:1024 sc1 lds\n" \
  "v_add_u32 v179, v179, v250\n" \
  "buffer_load_dwordx4 v178, %[rb], %[gso] offen offset:2048 sc1 lds\n" \
  "buffer_load_dwordx4 v179, %[rb], %[gso] offen offset:3072 sc1 lds\n" \
  "v_mfma_f32_32x32x16_bf16 v[32:47], v[148:151], v[208:211], v[32:47]\n" \
  "v_exp_f32 v120, v56\n" \
  "v_exp_f32 v121, v57\n" \
  "v_exp_f32 v122, v58\n" \
  "v_exp_f32 v123, v59\n" \
  "v_exp_f32 v124, v60\n" \
  "v_exp_f32 v125, v61\n" \
  "v_exp_f32 v126, v62\n" \
  "v_exp_f32 v127, v63\n" \
  "v_pk_add_f32 v[120:121], v[120:121], 1.0 op_sel_hi:[1,0]\n" \
  "v_pk_add_f32 v[122:123], v[122:123], 1.0 op_sel_hi:[1,0]\n" \
  "v_pk_add_f32 v[124:125], v[124:125], 1.0 op_sel_hi:[1,0]\n" \
  "v_pk_add_f32 v[126:127], v[126:127], 1.0 op_sel_hi:[1,0]\n" \
  "v_rcp_f32 v120, v120\n" \
  "v_rcp_f32 v121, v121\n" \
  "v_mfma_f32_32x32x16_bf16 v[96:111], v[220:223], v[228:231], v[96:111]\n" \
  "v_add_u32 v240, 0xcc0, v240\n" \
  "v_add_u32 v241, 0xcc0, v241\n" \
  "v_add_u32 v242, 0xcc0, v242\n" \
  "v_rcp_f32 v122, v122\n" \
  "v_rcp_f32 v123, v123\n" \
  "v_mfma_f32_32x32x16_bf16 v[64:79], v[128:131], v[168:171], 0\n" \
  "v_rcp_f32 v124, v124\n" \
  "v_rcp_f32 v125, v125\n" \
  "v_mfma_f32_32x32x16_bf16 v[96:111], v[224:227], v[232:235], v[96:111]\n" \
  "v_rcp_f32 v126, v126\n" \
  "v_rcp_f32 v127, v127\n" \
  "v_mfma_f32_32x32x16_bf16 v[64:79], v[132:135], v[172:175], v[64:79]\n" \
  "v_mfma_f32_32x32x16_bf16 v[80:95], v[236:239], v[236:239], 0\n" \
  "v_add_u32 v253, 0x1000, v253\n" \
  "v_pk_mul_f32 v[120:121], v[120:121], v[56:57]\n" \
  "v_pk_mul_f32 v[122:123], v[122:123], v[58:59]\n" \
  "v_pk_mul_f32 v[124:125], v[124:125], v[60:61]\n" \
  "v_pk_mul_f32 v[126:127], v[126:127], v[62:63]\n" \
  "v_xor_b32 v253, 0xc0, v253\n" \
  "v_cvt_pk_bf16_f32 v140, v120, v121\n" \
  "v_cvt_pk_bf16_f32 v141, v122, v123\n" \
  "v_cvt_pk_bf16_f32 v142, v124, v125\n" \
  "v_cvt_pk_bf16_f32 v143, v126, v127\n" \
  "s_nop 0\n" \
  "v_mul_f32 v28, 0xbf317218, v96\n" \
  "s_mov_b64 exec, s[62:63]\n" \
  "ds_write_b32 v254, v28\n" \
  "s_mov_b64 exec, s[64:65]\n" \
  "ds_add_u32 v30, v31\n" \
  "s_mov_b64 exec, -1\n" \
  "v_add_u32 v254, 0x80, v254\n" \
  "s_cbranch_scc1 11b\n" \
  "s_branch 30b\n" \
  "14:\n" \
  "s_lshr_b32 s66, s66, 1\n" \
  "v_mfma_f32_32x32x16_bf16 v[48:63], v[160:163], v[184:187], 0\n" \
  "v_add_u32 v251, 0x1000, v251\n" \
  "v_add_u32 v252, 0x1000, v252\n" \
  "v_xor_b32 v251, 0x80, v251\n" \
  "v_xor_b32 v252, 0x80, v252\n" \
  "ds_read_b128 v[196:199], v251\n" \
  "ds_read_b128 v[200:203], v252\n" \
  "v_mfma_f32_32x32x16_bf16 v[48:63], v[164:167], v[192:195], v[48:63]\n" \
  "v_cvt_pk_bf16_f32 v228, v64, v65\n" \
  "v_cvt_pk_bf16_f32 v229, v66, v67\n" \
  "v_cvt_pk_bf16_f32 v230, v68, v69\n" \
  "v_cvt_pk_bf16_f32 v231, v70, v71\n" \
  "v_cvt_pk_bf16_f32 v232, v72, v73\n" \
  "v_cvt_pk_bf16_f32 v233, v74, v75\n" \
  "v_cvt_pk_bf16_f32 v234, v76, v77\n" \
  "v_cvt_pk_bf16_f32 v235, v78, v79\n" \
  "v_mfma_f32_32x32x16_bf16 v[48:63], v[152:155], v[204:207], v[48:63]\n" \
  "v_xor_b32 v28, 16, v253\n" \
  "v_xor_b32 v29, 32, v253\n" \
  "v_xor_b32 v255, 48, v253\n" \
  "s_mov_b64 exec, s[60:61]\n" \
  "ds_write_b64 v253, v[228:229]\n" \
  "ds_write_b64 v28, v[230:231]\n" \
  "ds_write_b64 v29, v[232:233]\n" \
  "ds_write_b64 v255, v[234:235]\n" \
  "s_mov_b64 exec, -1\n" \
  "v_mfma_f32_32x32x16_bf16 v[48:63], v[156:159], v[208:211], v[48:63]\n" \
  "v_exp_f32 v124, v44\n" \
  "v_exp_f32 v125, v45\n" \
  "v_exp_f32 v126, v46\n" \
  "v_exp_f32 v127, v47\n" \
  "v_pk_add_f32 v[124:125], v[124:125], 1.0 op_sel_hi:[1,0]\n" \
  "v_pk_add_f32 v[126:127], v[126:127], 1.0 op_sel_hi:[1,0]\n" \
  "v_rcp_f32 v124, v124\n" \
  "v_rcp_f32 v125, v125\n" \
  "v_mfma_f32_32x32x16_bf16 v[80:95], v[136:139], v[168:171], v[80:95]\n" \
  "s_waitcnt vmcnt(0) lgkmcnt(0)\n" \
  "s_mov_b64 exec, s[76:77]\n" \
  "ds_add_u32 v30, v31 offset:24\n" \
  "s_mov_b64 exec, -1\n" \
  "ds_read_b128 v[160:163], v240 offset:5312\n" \
  "ds_read_b64_tr_b16 v[164:165], v241 offset:3264\n" \
  "ds_read_b64_tr_b16 v[166:167], v241 offset:3328\n" \
  "ds_read_b128 v[144:147], v243\n" \
  "ds_read_b128 v[148:151], v244\n" \
  "ds_read_b128 v[152:155], v245\n" \
  "ds_read_b128 v[156:159], v246\n" \
  "ds_read_b128 v[176:179], v242 offset:6528\n" \
  "v_mfma_f32_32x32x16_bf16 v[80:95], v[140:143], v[172:175], v[80:95]\n" \
  "ds_read_b128 v[168:171], v240\n" \
  "ds_read_b128 v[172:175], v240 offset:1024\n" \
  "v_rcp_f32 v126, v126\n" \
  "v_rcp_f32 v127, v127\n" \
  "v_mfma_f32_32x32x16_bf16 v[96:111], v[212:215], v[228:231], 0\n" \
  "s_nop 3\n" \
  "v_mfma_f32_32x32x16_bf16 v[96:111], v[216:219], v[232:235], v[96:111]\n" \
  "v_pk_mul_f32 v[124:125], v[124:125], v[44:45]\n" \
  "v_pk_mul_f32 v[126:127], v[126:127], v[46:47]\n" \
  "v_cvt_pk_bf16_f32 v134, v124, v125\n" \
  "v_cvt_pk_bf16_f32 v135, v126, v127\n" \
  "s_waitcnt lgkmcnt(0)\n" \
  "s_cbranch_scc1 24f\n" \
  "v_cndmask_b32 v188, v26, v196, vcc\n" \
  "v_cndmask_b32 v189, 0, v197, vcc\n" \
  "v_cndmask_b32 v190, 0, v198, vcc\n" \
  "v_cndmask_b32 v191, 0, v199, vcc\n" \
  "24:\n" \
  "v_mfma_f32_32x32x16_bf16 v[32:47], v[160:163], v[180:183], 0\n" \
  "s_nop 2\n" \
  "v_cvt_pk_bf16_f32 v228, v80, v81\n" \
  "v_cvt_pk_bf16_f32 v229, v82, v83\n" \
  "v_cvt_pk_bf16_f32 v230, v84, v85\n" \
  "v_cvt_pk_bf16_f32 v231, v86, v87\n" \
  "v_cvt_pk_bf16_f32 v232, v88, v89\n" \
  "v_cvt_pk_bf16_f32 v233, v90, v91\n" \
  "v_cvt_pk_bf16_f32 v234, v92, v93\n" \
  "v_cvt_pk_bf16_f32 v235, v94, v95\n" \
  "v_mfma_f32_32x32x16_bf16 v[32:47], v[164:167], v[188:191], v[32:47]\n" \
  "v_xor_b32 v253, 64, v253\n" \
  "v_xor_b32 v28, 16, v253\n" \
  "v_xor_b32 v29, 32, v253\n" \
  "v_xor_b32 v255, 48, v253\n" \
  "s_mov_b64 exec, s[60:61]\n" \
  "ds_write_b64 v253, v[228:229]\n" \
  "ds_write_b64 v28, v[230:231]\n" \
  "ds_write_b64 v29, v[232:233]\n" \
  "ds_write_b64 v255, v[234:235]\n" \
  "s_mov_b64 exec, -1\n" \
  "v_mfma_f32_32x32x16_bf16 v[32:47], v[144:147], v[204:207], v[32:47]\n" \
  "v_add_u32 v176, v176, v247\n" \
  "v_add_u32 v177, v177, v248\n" \
  "buffer_load_dwordx4 v176, %[rb], %[gso] offen sc1 lds\n" \
  "v_add_u32 v178, v178, v249\n" \
  "buffer_load_dwordx4 v177, %[rb], %[gso] offen offset:1024 sc1 lds\n" \
  "v_add_u32 v179, v179, v250\n" \
  "buffer_load_dwordx4 v178, %[rb], %[gso] offen offset:2048 sc1 lds\n" \
  "buffer_load_dwordx4 v179, %[rb], %[gso] offen offset:3072 sc1 lds\n" \
  "v_mfma_f32_32x32x16_bf16 v[32:47], v[148:151], v[208:211], v[32:47]\n" \
  "v_exp_f32 v124, v60\n" \
  "v_exp_f32 v125, v61\n" \
  "v_exp_f32 v126, v62\n" \
  "v_exp_f32 v127, v63\n" \
  "v_pk_add_f32 v[124:125], v[124:125], 1.0 op_sel_hi:[1,0]\n" \
  "v_pk_add_f32 v[126:127], v[126:127], 1.0 op_sel_hi:[1,0]\n" \
  "v_rcp_f32 v124, v124\n" \
  "v_rcp_f32 v125, v125\n" \
  "v_mfma_f32_32x32x16_bf16 v[96:111], v[220:223], v[228:231], v[96:111]\n" \
  "v_add_u32 v240, 0xcc0, v240\n" \
  "v_add_u32 v241, 0xcc0, v241\n" \
  "v_add_u32 v242, 0xcc0, v242\n" \
  "v_rcp_f32 v126, v126\n" \
  "v_rcp_f32 v127, v127\n" \
  "v_mfma_f32_32x32x16_bf16 v[64:79], v[128:131], v[168:171], 0\n" \
  "v_mfma_f32_32x32x16_bf16 v[96:111], v[224:227], v[232:235], v[96:111]\n" \
  "v_mfma_f32_32x32x16_bf16 v[64:79], v[132:135], v[172:175], v[64:79]\n" \
  "v_mfma_f32_32x32x16_bf16 v[80:95], v[236:239], v[236:239], 0\n" \
  "v_add_u32 v253, 0x1000, v253\n" \
  "v_pk_mul_f32 v[124:125], v[124:125], v[60:61]\n" \
  "v_pk_mul_f32 v[126:127], v[126:127], v[62:63]\n" \
  "v_xor_b32 v253, 0xc0, v253\n" \
  "v_cvt_pk_bf16_f32 v142, v124, v125\n" \
  "v_cvt_pk_bf16_f32 v143, v126, v127\n" \
  "s_nop 3\n" \
  "v_mul_f32 v28, 0xbf317218, v96\n" \
  "s_mov_b64 exec, s[62:63]\n" \
  "ds_write_b32 v254, v28\n" \
  "s_mov_b64 exec, s[64:65]\n" \
  "ds_add_u32 v30, v31\n" \
  "s_mov_b64 exec, -1\n" \
  "v_add_u32 v254, 0x80, v254\n" \
  "s_cbranch_scc1 11b\n" \
  "s_branch 30b\n" \
  "40:\n" \
  "v_mfma_f32_32x32x16_bf16 v[80:95], v[136:139], v[168:171], v[80:95]\n" \
  "s_waitcnt lgkmcnt(0)\n" \
  "s_nop 0\n" \
  "v_cvt_pk_bf16_f32 v228, v64, v65\n" \
  "v_cvt_pk_bf16_f32 v229, v66, v67\n" \
  "v_cvt_pk_bf16_f32 v230, v68, v69\n" \
  "v_cvt_pk_bf16_f32 v231, v70, v71\n" \
  "v_mfma_f32_32x32x16_bf16 v[80:95], v[140:143], v[172:175], v[80:95]\n" \
  "v_cvt_pk_bf16_f32 v232, v72, v73\n" \
  "v_cvt_pk_bf16_f32 v233, v74, v75\n" \
  "v_cvt_pk_bf16_f32 v234, v76, v77\n" \
  "v_cvt_pk_bf16_f32 v235, v78, v79\n" \
  "v_xor_b32 v28, 16, v253\n" \
  "v_xor_b32 v29, 32, v253\n" \
  "v_xor_b32 v255, 48, v253\n" \
  "s_mov_b64 exec, s[60:61]\n" \
  "ds_write_b64 v253, v[228:229]\n" \
  "ds_write_b64 v28, v[230:231]\n" \
  "ds_write_b64 v29, v[232:233]\n" \
  "ds_write_b64 v255, v[234:235]\n" \
  "s_mov_b64 exec, -1\n" \
  "v_mfma_f32_32x32x16_bf16 v[96:111], v[212:215], v[228:231], 0\n" \
  "v_xor_b32 v253, 64, v253\n" \
  "v_mfma_f32_32x32x16_bf16 v[96:111], v[216:219], v[232:235], v[96:111]\n" \
  "v_cvt_pk_bf16_f32 v228, v80, v81\n" \
  "v_cvt_pk_bf16_f32 v229, v82, v83\n" \
  "v_cvt_pk_bf16_f32 v230, v84, v85\n" \
  "v_cvt_pk_bf16_f32 v231, v86, v87\n" \
  "v_cvt_pk_bf16_f32 v232, v88, v89\n" \
  "v_cvt_pk_bf16_f32 v233, v90, v91\n" \
  "v_cvt_pk_bf16_f32 v234, v92, v93\n" \
  "v_cvt_pk_bf16_f32 v235, v94, v95\n" \
  "v_xor_b32 v28, 16, v253\n" \
  "v_xor_b32 v29, 32, v253\n" \
  "v_xor_b32 v255, 48, v253\n" \
  "s_mov_b64 exec, s[60:61]\n" \
  "ds_write_b64 v253, v[228:229]\n" \
  "ds_write_b64 v28, v[230:231]\n" \
  "ds_write_b64 v29, v[232:233]\n" \
  "ds_write_b64 v255, v[234:235]\n" \
  "s_mov_b32 s64, %[sig]\n" \
  "s_mov_b64 exec, s[64:65]\n" \
  "ds_add_u32 v30, v31 offset:4\n" \
  "s_mov_b64 exec, -1\n" \
  "v_mfma_f32_32x32x16_bf16 v[96:111], v[220:223], v[228:231], v[96:111]\n" \
  "v_add_u32 v30, %[fin], v30\n" \
  "s_mov_b32 s64, 1\n" \
  "v_mfma_f32_32x32x16_bf16 v[96:111], v[224:227], v[232:235], v[96:111]\n" \
  "s_nop 11\n" \
  "v_mul_f32 v28, 0xbf317218, v96\n" \
  "s_mov_b64 exec, s[62:63]\n" \
  "ds_write_b32 v254, v28\n" \
  "s_mov_b64 exec, s[64:65]\n" \
  "ds_add_u32 v30, v31\n" \
  "s_mov_b64 exec, -1\n"
#define EDGE_CLOB \
  "v29", "v31", "v32", "v33", "v34", "v35", "v36", "v37", "v38", "v39", "v40", "v41", "v42", "v43", "v44", "v45", "v46", "v47", "v48", "v49", "v50", \
  "v51", "v52", "v53", "v54", "v55", "v56", "v57", "v58", "v59", "v60", "v61", "v62", "v63", "v64", "v65", "v66", "v67", "v68", "v69", "v70", "v71", \
  "v72", "v73", "v74", "v75", "v76", "v77", "v78", "v79", "v80", "v81", "v82", "v83", "v84", "v85", "v86", "v87", "v88", "v89", "v90", "v91", "v92", \
  "v93", "v94", "v95", "v96", "v97", "v98", "v99", "v100", "v101", "v102", "v103", "v104", "v105", "v106", "v107", "v108", "v109", "v110", "v111", \
  "v112", "v113", "v114", "v115", "v116", "v117", "v118", "v119", "v120", "v121", "v122", "v123", "v124", "v125", "v126", "v127", "v128", "v129", \
  "v130", "v131", "v132", "v133", "v134", "v135", "v136", "v137", "v138", "v139", "v140", "v141", "v142", "v143", "v144", "v145", "v146", "v147", \
  "v148", "v149", "v150", "v151", "v152", "v153", "v154", "v155", "v156", "v157", "v158", "v159", "v160", "v161", "v162", "v163", "v164", "v165", \
  "v166", "v167", "v168", "v169", "v170", "v171", "v172", "v173", "v174", "v175", "v176", "v177", "v178", "v179", "v188", "v189", "v190", "v191", \
  "v192", "v193", "v194", "v195", "v196", "v197", "v198", "v199", "v200", "v201", "v202", "v203", "v212", "v213", "v214", "v215", "v216", "v217", \
  "v218", "v219", "v220", "v221", "v222", "v223", "v224", "v225", "v226", "v227", "v228", "v229", "v230", "v231", "v232", "v233", "v234", "v235", \
  "v236", "v237", "v238", "v239", "v244", "v245", "v246", "v248", "v249", "v250", "v255", "s60", "s61", "s62", "s63", "s64", "s65", "s66", "s67", "s68", \
  "s69", "s70", "s71", "s72", "s73", "s74", "s75", "s76", "s77", "vcc", "scc", "memory"
DI void gd4(u32x4 s, u32 c0, u32 c1, u32 c2, u32 c3, __amdgpu_buffer_rsrc_t r, u32 soff) {
  u32 a = s[0], b = s[1], c = s[2], d = s[3];
  asm volatile("v_add_u32 %0, %0, %4\n\tv_add_u32 %1, %1, %5\n\tbuffer_load_dwordx4 %0, %8, %9 offen sc1 lds\n\tv_add_u32 %2, %2, %6\n\t"
               "buffer_load_dwordx4 %1, %8, %9 offen offset:1024 sc1 lds\n\tv_add_u32 %3, %3, %7\n\t"
               "buffer_load_dwordx4 %2, %8, %9 offen offset:2048 sc1 lds\n\tbuffer_load_dwordx4 %3, %8, %9 offen offset:3072 sc1 lds"
               : "+v"(a), "+v"(b), "+v"(c), "+v"(d) : "v"(c0), "v"(c1), "v"(c2), "v"(c3), "s"(r), "s"(soff) : "memory");
}
#define MF32V(c, a, b) asm volatile("v_mfma_f32_32x32x16_bf16 %0, %1, %2, %0" : "+v"(c) : "v"(a), "v"(b))
#define MF16V(c, a, b) asm volatile("v_mfma_f32_16x16x32_bf16 %0, %1, %2, %0" : "+v"(c) : "v"(a), "v"(b))
#define MF_BEGIN(a, b, c, d) asm volatile("s_nop 1" : "+v"(a), "+v"(b), "+v"(c), "+v"(d))
#define MF_END(a, b, c, d) asm volatile("s_nop 15" : "+v"(a), "+v"(b), "+v"(c), "+v"(d))
template <int NOP, class F>
DI void kloop(const char* smem, u32 a0, u32 a1, F step) {
  constexpr int PF = NOP > 1 ? 3 : 4;
  bf16x8 x[16], y[16];
#pragma unroll
  for (int s = 0; s < PF; ++s) { x[s] = ld128(smem, (a0 ^ (32 * (s & 7))) + 256 * (s >> 3)); if (NOP > 1) y[s] = ld128(smem, (a1 ^ (32 * (s & 7))) + 256 * (s >> 3)); }
  SB();
#pragma unroll
  for (int s = 0; s < 16; ++s) {
    if (s + PF < 16) {
      x[s + PF] = ld128(smem, (a0 ^ (32 * ((s + PF) & 7))) + 256 * ((s + PF) >> 3));
      if (NOP > 1) y[s + PF] = ld128(smem, (a1 ^ (32 * ((s + PF) & 7))) + 256 * ((s + PF) >> 3));
    }
    SB();
    step(s, x[s], y[s]);
    SB();
  }
}
__global__ __launch_bounds__(512, 2) void egnn(const u16* __restrict__ h, const u16* __restrict__ pos, const u16* __restrict__ wh, const u16* __restrict__ ea, const int* __restrict__ srcg,
                                          const int* __restrict__ dstg, const u16* __restrict__ we, const u16* __restrict__ wxg,
                                          char* ws, u16* out) {
  __shared__ __attribute__((aligned(1024))) char smem[LDS2];
  const int tid = threadIdx.x, lane = tid & 63, hh = lane >> 5, r31 = lane & 31;
  const int w = __builtin_amdgcn_readfirstlane(tid >> 6), q = w & 3, p = w >> 2;
  const int b = blockIdx.x, node0 = bucket_base(b), nend = bucket_base(b + 1);
  const __amdgpu_buffer_rsrc_t rO = mk_rsrc(out, nend * 512);
  const u32 sb = lds_addr(smem);
  const __amdgpu_buffer_rsrc_t rH = mk_rsrc(h, N * 512), rB = mk_rsrc(ws + WS_B - 4096, N * 512 + 4096), rE = mk_rsrc(ws + WS_EA, E * 32);
  u32* gcnt = (u32*)(smem + O_CNT);
  float* pdp = (float*)(smem + O_PD);
  const __amdgpu_buffer_rsrc_t rP = mk_rsrc(ws + WS_POS4, N * 8), rF = mk_rsrc(ws + WS_FL, 352 * 128), rS4 = mk_rsrc(ws + WS_W4F, 9728 + N * 8);
  if (lane == 0) dma16f(sb + O_EP + w * 16, 0, rB, 0);
  auto mktag = [&]() {
    const u32x2 e = *(const u32x2*)(smem + O_EP + w * 16);
    const unsigned long long t = (((unsigned long long)(u32)__builtin_amdgcn_readfirstlane(e[1]) << 32) | (u32)__builtin_amdgcn_readfirstlane(e[0])) + 1;
    return u32x2{(u32)t, (u32)(t >> 32)};
  };
  u32x2 tag;
  if (tid >= 448 && tid < 488) *(u32x4*)(smem + O_Z + (tid - 448) * 16) = u32x4{0, 0, 0, 0};
  auto hrows = [&](int k) {
    const u32 row = 2 * k + hh;
    const u32 vh = (node0 + row) * 512 + 16 * (r31 ^ row), vh2 = (node0 + row + 8) * 512 + 16 * (r31 ^ (row + 8));
    dma16f(sb + O_H + k * 1024, vh, rH, 0); dma16f(sb + O_H + k * 1024 + 4096, vh2, rH, 0);
    dma16f(sb + O_H + k * 1024 + 8192, vh, rH, 8192); dma16f(sb + O_H + k * 1024 + 12288, vh2, rH, 8192);
    dma16f(sb + O_H + k * 1024 + 16384, vh, rH, 16384);
  };
  if (b < 160 && p) hrows(q);
  int slot = -1;
  const bool binr = b >= 192;
  {
  const int bid = b < 160 ? b : b < 192 ? b + 64 : b - 32;
  if (bid < 160) {
    const int tile = (bid >> 4) * 8 + (bid & 7), half = (bid >> 3) & 1;
    if (tile < 79) {
    const int node0 = tile * 128;
    const __amdgpu_buffer_rsrc_t rW = mk_rsrc(we + 256 * 256, 256 * 512);
    {
      const u32 rq = lane >> 4;
      const u32 vW = (8 * w + rq) * 512 + half * 256 + 16 * ((lane & 15) ^ (((rq & 1) << 1) | ((rq & 2) << 2)));
      const u32 hr = node0 + 16 * w + (lane >> 3);
      const u32 vH0 = hr * 512 + 16 * ((lane & 7) ^ rq), vH1 = (hr + 8) * 512 + 16 * ((lane & 7) ^ (4 + rq));
#pragma unroll
      for (int st = 0; st < 4; ++st) {
        dma16(sb + st * 16384 + w * 2048, vW, rW, st * 32768);
        dma16(sb + st * 16384 + w * 2048 + 1024, vW, rW, st * 32768 + 2048);
        dma16(sb + 65536 + st * 16384 + w * 2048, vH0, rH, st * 128);
        dma16(sb + 65536 + st * 16384 + w * 2048 + 1024, vH1, rH, st * 128);
      }
    }
    const int fq = w & 1, nt = w >> 1;
    u32 aw[2];
    {
      const u32 blk = (lane >> 4) & 1, q = (lane & 15) >> 2, p = lane & 3, u = 4 * blk + p;
      const u32 fw = ((q & 1) << 1) | ((q & 2) << 2);
#pragma unroll
      for (int m = 0; m < 2; ++m) {
        const u32 v = 8 * (u & 1) + 4 * m + (u >> 1), ch = 8 * fq + (v >> 1);
        aw[m] = 256 * (8 * hh + q) + 16 * (ch ^ fw) + 8 * (v & 1);
      }
    }
    const u32 hrow = 32 * nt + (lane & 31);
    const u32 bh = 65536 + hrow * 128 + 16 * (hh ^ ((hrow >> 1) & 7));
    f32x16 acc0 = {}, acc1 = {};
    bf16x8 fa[16][2], fb[16];
    auto rdk = [&](int ks) {
      fa[ks][0] = tr2(smem, aw[0] + 4096 * ks, aw[0] + 4096 * ks + 1024); fa[ks][1] = tr2(smem, aw[1] + 4096 * ks, aw[1] + 4096 * ks + 1024);
      fb[ks] = ld128(smem, (bh ^ (32 * (ks & 3))) + 16384 * (ks >> 2));
    };
    asm volatile("s_waitcnt vmcnt(12)\n\ts_barrier" ::: "memory");
    rdk(0); rdk(1);
#pragma unroll
    for (int ks = 0; ks < 16; ++ks) {
      if (ks == 2) asm volatile("s_waitcnt vmcnt(8)\n\ts_barrier" ::: "memory");
      if (ks == 6) asm volatile("s_waitcnt vmcnt(4)\n\ts_barrier" ::: "memory");
      if (ks == 10) asm volatile("s_waitcnt vmcnt(0)\n\ts_barrier" ::: "memory");
      if (ks + 2 < 16) rdk(ks + 2);
      SB();
      acc0 = MFMA32(fa[ks][0], fb[ks], acc0);
      acc1 = MFMA32(fa[ks][1], fb[ks], acc1);
      SB();
    }
    {
      char* tb = smem + w * 4096;
      const u32 r = lane & 31, wa = r * 128 + 16 * ((4 * hh) ^ (r & 7));
      u32x4 v;
#pragma unroll
      for (int j = 0; j < 4; ++j) {
#pragma unroll
        for (int i = 0; i < 4; ++i)
          v[i] = j < 2 ? pk2(acc0[8 * j + 2 * i] * SCL, acc0[8 * j + 2 * i + 1] * SCL) : pk2(acc1[8 * j - 16 + 2 * i] * SCL, acc1[8 * j - 15 + 2 * i] * SCL);
        *(u32x4*)(tb + (wa ^ (16 * j))) = v;
      }
      BAR_LGKM();
      if (w < 4) {
        const __amdgpu_buffer_rsrc_t rT = mk_rsrc(ws + WS_B, N * 512);
        const u32 rr = lane >> 3, ra = rr * 128 + 16 * ((lane & 7) ^ rr);
        const u32 oo = (node0 + 32 * nt + rr) * 512 + half * 256 + fq * 128 + (lane & 7) * 16;
        u32x4 o[8];
#pragma unroll
        for (int j = 0; j < 8; ++j) o[j] = *(const u32x4*)(tb + ra + 1024 * (j & 3) + 16384 * (j >> 2));
#pragma unroll
        for (int j = 0; j < 8; ++j) __builtin_amdgcn_raw_buffer_store_b128(o[j], rT, oo + 4096 * (j & 3) + 32768 * (j >> 2), 0, 16);
      }
    }
    }
    slot = tile < 79 ? 192 + bid : -1;
  } else if (bid < 224) {
    const int c = bid - 160;
    u32* cnt = (u32*)smem;
    u32* pre = (u32*)(smem + 1024);
    u16* inv = (u16*)(smem + 2048);
    u32* stage = (u32*)(smem + 7168);
    constexpr u32 O_EA = 44032;
    const bool has2 = tid < CHE / 4 - 384;
    const int i2 = has2 ? 384 + tid : tid;
    u32x4 dv0, sv0, dv1, sv1;
    u32 bb[8], rk[8];
    if (tid < 256) cnt[tid] = 0;
    if (tid == 256) *(u32x2*)(smem + 7160) = u32x2{0, 0};
    if (w >= 6) {
      const __amdgpu_buffer_rsrc_t rS = mk_rsrc(ea + (size_t)c * CHE * 16, CHE * 32);
      BAR_LGKM();
      for (int i = w - 6; i < 79; i += 2) dma16n(lds_addr(smem) + O_EA + i * 1024, lane * 16, rS, i * 1024);
      hrows(2 * w - 12); hrows(2 * w - 11);
    } else {
      const int* dp = dstg + c * CHE;
      const int* sp = srcg + c * CHE;
      asm volatile("global_load_dwordx4 %0, %4, %6 nt\n\tglobal_load_dwordx4 %2, %5, %6 nt\n\tglobal_load_dwordx4 %1, %4, %7 nt\n\tglobal_load_dwordx4 %3, %5, %7 nt\n\t"
                   "s_waitcnt lgkmcnt(0)\n\ts_barrier\n\ts_waitcnt vmcnt(0)"
                   : "=&v"(dv0), "=&v"(sv0), "=&v"(dv1), "=&v"(sv1) : "v"(tid * 16), "v"(i2 * 16), "s"(dp), "s"(sp) : "memory");
    }
    const u32 dd[8] = {dv0[0], dv0[1], dv0[2], dv0[3], dv1[0], dv1[1], dv1[2], dv1[3]}, ss[8] = {sv0[0], sv0[1], sv0[2], sv0[3], sv1[0], sv1[1], sv1[2], sv1[3]};
    u32* sy = (u32*)(smem + 7164);
    if (w < 6) {
      u32 ix[8], ent[8];
      auto rank4 = [&](int k0) {
#pragma unroll
        for (int k = k0; k < k0 + 4; ++k) {
          bb[k] = (u32)max((int)((u32)__umul24(dd[k], 52429u) >> 21), (int)((u32)__umul24(4 * dd[k] + 23, 57853u) >> 23) - 20);
          ix[k] = ((bb[k] & 7) * 32 + (bb[k] >> 3)) * 4;
          rk[k] = atomicAdd((u32*)(smem + ix[k]), 1u);
          ent[k] = ((dd[k] - min((u32)__umul24(bb[k], 40u), ((u32)__umul24(bb[k], 145u) >> 2) + 720)) << 26) | (ss[k] << 12);
        }
      };
      rank4(0);
      if (has2) rank4(4);
      lds_signal(sy, lane);
      lds_wait<0>(sy, 6);
      const u32x4 v = *(const u32x4*)&cnt[4 * lane];
      const u32 s1 = v[0] + v[1], s2 = s1 + v[2], tot = s2 + v[3];
      int inc = (int)tot;
      inc += __builtin_amdgcn_update_dpp(0, inc, 0x111, 0xf, 0xf, false);
      inc += __builtin_amdgcn_update_dpp(0, inc, 0x112, 0xf, 0xf, false);
      inc += __builtin_amdgcn_update_dpp(0, inc, 0x114, 0xf, 0xf, false);
      inc += __builtin_amdgcn_update_dpp(0, inc, 0x118, 0xf, 0xf, false);
      inc += __builtin_amdgcn_update_dpp(0, inc, 0x142, 0xa, 0xf, false);
      inc += __builtin_amdgcn_update_dpp(0, inc, 0x143, 0xc, 0xf, false);
      const u32 ex = (u32)inc - tot;
      *(u32x4*)&pre[4 * lane] = u32x4{ex, ex + v[0], ex + s1, ex + s2};
      auto put4 = [&](int k0, u32 e0) {
        u32 ps[4];
#pragma unroll
        for (int k = 0; k < 4; ++k) ps[k] = *(const u32*)(smem + 1024 + ix[k0 + k]);
#pragma unroll
        for (int k = 0; k < 4; ++k) {
          const u32 pp = ps[k] + rk[k0 + k], r1 = rk[k0 + k] + 1;
          inv[pp] = (u16)(e0 + k);
          stage[bb[k0 + k] * 36 + (r1 < 32 ? (r1 & 3) * 8 + (r1 >> 2) : 0)] = ent[k0 + k] | pp;
        }
      };
      put4(0, 4 * tid);
      if (has2) put4(4, 4 * i2);
    }
    if (w < 6) lds_signal(sy, lane);
    lds_wait<0>(sy, 12);
    if (w < 6) {
      const __amdgpu_buffer_rsrc_t rC = mk_rsrc(ws + WS_CELL, NB * NCH * 128);
      u32x4 v[6]; u32 cb[6];
#pragma unroll
      for (int it = 0; it < 6; ++it) {
        const int bk = min((tid + 384 * it) >> 3, NB - 1);
        v[it] = *(const u32x4*)&stage[bk * 36 + 4 * (tid & 7)];
        cb[it] = cnt[(bk & 7) * 32 + (bk >> 3)];
      }
#pragma unroll
      for (int it = 0; it < 6; ++it) {
        const int i = tid + 384 * it;
        v[it][0] = (tid & 7) ? v[it][0] : cb[it];
        __builtin_amdgcn_raw_buffer_store_b128(v[it], rC, i < NB * 8 ? (((i >> 3) * NCH + c) * 8 + (tid & 7)) * 16 : OOB, 0, 16);
      }
    }
    u32 rows[10];
#pragma unroll
    for (int i = 0; i < 10; ++i) rows[i] = inv[min((tid + 512 * i) >> 1, CHE - 1)];
    tag = mktag();
    asm volatile("s_waitcnt vmcnt(0)" ::: "memory");
    if (w < 6) { publish<6>(&gcnt[17], rF, c, tag, lane); } else lds_signal(sy - 1, lane);
    lds_wait<0>(sy - 1, 2);
    {
      const __amdgpu_buffer_rsrc_t rT = mk_rsrc(ws + WS_EA + (size_t)c * CHE * 32, CHE * 32);
      u32x4 ev[10];
#pragma unroll
      for (int i = 0; i < 10; ++i) ev[i] = *(const u32x4*)(smem + O_EA + rows[i] * 32 + (tid & 1) * 16);
#pragma unroll
      for (int i = 0; i < 10; ++i) __builtin_amdgcn_raw_buffer_store_b128(ev[i], rT, tid * 16, i * 8192, 16);
    }
    slot = 128 + c;
  } else if (bid < 248) {
    slot = 64 + bid - 224;
#pragma unroll
    for (int it = 0; it < 2; ++it) {
      const int x = (bid - 224) * 1024 + 512 * it + tid;
      const u16* p;
      if (x < 8192) {
        const int ct = x >> 10, s = (x >> 6) & 15;
        p = we + (16 * s + 8 * (lane >> 5)) * 256 + 32 * ct + (lane & 31);
      } else {
        const int y = x - 8192, mt = y >> 11, s = (y >> 6) & 31, r = lane & 31;
        p = wh + (16 * s + 8 * (lane >> 5)) * 256 + 64 * (mt >> 1) + 32 * ((r >> 2) & 1) + 16 * (mt & 1) + 4 * (r >> 3) + (r & 3);
      }
      const float sc = (x >= 8192 && !((x >> 10) & 1)) ? SCL : 1.0f;
      u32x4 v;
#pragma unroll
      for (int j = 0; j < 4; ++j) v[j] = pk2(bf2f(p[(2 * j) * 256]) * sc, bf2f(p[(2 * j + 1) * 256]) * sc);
      __builtin_amdgcn_raw_buffer_store_b128(v, mk_rsrc(ws + WS_W1F, 393216), x * 16, 0, 16);
    }
  } else if (bid == 248) {
    slot = 88;
    {
      const u16* p = we + (513 + 8 * (lane >> 5)) * 256 + 32 * w + (lane & 31);
      u32x4 v;
#pragma unroll
      for (int j = 0; j < 4; ++j) v[j] = pk2(bf2f(p[(2 * j) * 256]) * SCL, bf2f(p[(2 * j + 1) * 256]) * SCL);
      __builtin_amdgcn_raw_buffer_store_b128(v, rS4, tid * 16, 0, 16);
    }
    if (tid < 256) {
      const float v = bf2f(we[512 * 256 + tid]) * SCL;
      const u32 hi = f2bf(v);
      __builtin_amdgcn_raw_buffer_store_b32(hi | (f2bf(v - bf2f(hi)) << 16), rS4, 8192 + tid * 4, 0, 16);
    }
    if (tid < 32) {
      const int f0 = 32 * (tid >> 2) + 16 * ((tid >> 1) & 1) + 4 * (tid & 1);
      u32x4 v;
      v[0] = (u32)wxg[f0] | ((u32)wxg[f0 + 1] << 16); v[1] = (u32)wxg[f0 + 2] | ((u32)wxg[f0 + 3] << 16);
      v[2] = (u32)wxg[f0 + 8] | ((u32)wxg[f0 + 9] << 16); v[3] = (u32)wxg[f0 + 10] | ((u32)wxg[f0 + 11] << 16);
      __builtin_amdgcn_raw_buffer_store_b128(v, rS4, 9216 + tid * 16, 0, 16);
    }
  } else if (bid < 253) {
    slot = 89 + bid - 249;
    const int n0 = (bid - 249) * 2500;
#pragma unroll
    for (int it = 0; it < 5; ++it) {
      const int i = tid + 512 * it;
      if (i < 2500) {
        const u16* p = pos + (n0 + i) * 3;
        u32x2 v = {(u32)p[0] | ((u32)p[1] << 16), (u32)p[2]};
        __builtin_amdgcn_raw_buffer_store_b64(v, rP, (n0 + i) * 8, 0, 16);
      }
    }
  }
  }
  BAR_LGKM();
  auto lds_init = [&]() {
    const u32x4 z = {0, 0, 0, 0}, oo = {OOB, OOB, OOB, OOB}, ff = {~0u, ~0u, ~0u, ~0u};
    const u32x4 zo = lane < 4 ? z : oo;
    char* tp = smem + w * TS + lane * 16;
#pragma unroll
    for (int i = 0; i < 4; ++i)
      if (i < 3 || w < TCAP - 24) {
        *(u32x4*)(tp + 8 * i * TS) = z; *(u32x4*)(tp + 8 * i * TS + 1024) = z;
        if (lane < 12) *(u32x4*)(tp + 8 * i * TS + T_D2) = zo;
      }
    if (tid < TCAP * 8) *(u32x4*)(smem + O_EL + tid * 16) = ff;
    BAR_LGKM();
    asm volatile("s_waitcnt vmcnt(0)" ::: "memory");
    tag = mktag();
    publish(&gcnt[16], rF, slot >= 0 ? slot : b < 160 ? 192 + b : b - 96, tag, lane);
  };
  auto axa = [&](u32 g, u32 qq, u32 x) { const u32 nd = 8 * g + (x >> 3); return O_M + nd * 512 + (qq >> 1) * 256 + 16 * ((8 * (qq & 1) + (x & 7)) ^ (nd & 15)); };
  const u32 gs0 = 16 * ((lane & 7) ^ (lane >> 4)) + 4096, gso = q * 128, gm0 = sb + O_G + w * 4096;
  bf16x8 W40, W41; u32 wd0, wd1;
  int ng[5], tb[6];
  bool slow;
  auto counts = [&]() {
    const u32x4 c0 = *(const u32x4*)(smem + O_CNT), c1 = *(const u32x4*)(smem + O_CNT + 16);
    ng[0] = __builtin_amdgcn_readfirstlane(c0[0]); ng[1] = __builtin_amdgcn_readfirstlane(c0[1]); ng[2] = __builtin_amdgcn_readfirstlane(c0[2]);
    ng[3] = __builtin_amdgcn_readfirstlane(c0[3]); ng[4] = __builtin_amdgcn_readfirstlane(c1[0]);
    tb[0] = 0;
#pragma unroll
    for (int g = 0; g < 5; ++g) tb[g + 1] = tb[g] + max((ng[g] + 31) >> 5, 1);
    slow = __builtin_amdgcn_readfirstlane(c1[3]) != 0 || tb[5] > TCAP;
  };
  auto put = [&](u32 s, u32 en, u32x2 psv) {
    const u32 dl = min(en >> 18, (u32)NPB - 1);
    const u32x2 pd = *(const u32x2*)(smem + O_POS + dl * 8);
    const u32 dxy = pk2(bf2f(pd[0] & 0xffff) - bf2f(psv[0] & 0xffff), bf2f(pd[0] >> 16) - bf2f(psv[0] >> 16));
    const u32 dz = f2bf(bf2f(pd[1] & 0xffff) - bf2f(psv[1] & 0xffff));
    const float fx = bf2f(dxy & 0xffff), fy = bf2f(dxy >> 16), fz = bf2f(dz);
    const u32 sxy = pk2(fx * fx, fy * fy), sz = f2bf(fz * fz);
    const u32 d2 = f2bf(bf2f(sxy & 0xffff) + bf2f(sxy >> 16) + bf2f(sz));
    const u32 sh = (s >> 2) & 1, ss = (s >> 4) & 1, o = sb + (s >> 5) * TS + 2 * ((s & 3) + 4 * ((s >> 3) & 1));
    unsigned long long sv;
    asm volatile("v_cmp_ne_u32 vcc, -1, %1\n\ts_and_saveexec_b64 %0, vcc\n\tds_write_b16 %2, %3\n\tds_write_b16 %2, %4 offset:128\n\tds_write_b16_d16_hi %2, %4 offset:256\n\t"
                 "ds_write_b16 %2, %5 offset:384\n\tds_write_b16 %6, %7 offset:3072\n\ts_mov_b64 exec, %0"
                 : "=&s"(sv) : "v"(en), "v"(o + ss * 1024 + sh * 512 + (dl & 7) * 16), "v"(0x3f80u), "v"(dxy), "v"(dz), "v"(o + ss * 32 + sh * 16), "v"(d2) : "vcc", "scc", "memory");
  };
  bf16x8 I0, I1;
  u32 srd, ga, la, gsv, tra, ax0, ax1, ma, pda, wxa, ca, nseq, nseqh, ngr, tmp, chk, once;
  auto inputs = [&]() {
    int lz = threadIdx.x & 63;
    asm volatile("" : "+v"(lz));
    const int hz = lz >> 5, rz = lz & 31, t0 = p ? tb[G0] : 0;
    {
      const u32 k = (rz & 15) - 8 * hz, one = 0x3f80u << (16 * (k & 1));
      u32x4 a, c;
#pragma unroll
      for (int i = 0; i < 4; ++i) { const u32 v = (k >> 1) == (u32)i ? one : 0u; a[i] = rz < 16 ? v : 0u; c[i] = rz < 16 ? 0u : v; }
      I0 = asbf(a); I1 = asbf(c);
    }
    const u32 g0 = p * G0, nd = 8 * g0 + rz;
    srd = sb + T_SRC + (lz >> 3) * 16 + t0 * TS;
    ga = sb + O_G + w * 4096 + rz * 128 + 16 * (hz ^ ((rz >> 1) & 7)); la = sb + lz * 16 + t0 * TS; gsv = 16 * ((lz & 7) ^ (lz >> 4)) + 4096;
    tra = sb + t0 * TS + 8 * ((lz >> 1) & 1) + (hz ? ((lz >> 4) & 1) * 1024 + (lz & 1) * 512 + ((lz >> 2) & 3) * 16 : T_D2 + ((lz >> 4) & 1) * 32 + (lz & 1) * 16);
    ax0 = sb + axa(g0, q, rz); ax1 = sb + axa(g0, q, 32 + rz);
    ma = sb + ((O_M + nd * 512 + 8 * hz + (q >> 1) * 256 + 16 * (((q & 1) * 8) ^ (nd & 15))) ^ 128) - 4096;
    pda = sb + O_PD + (q * 160 + g0 * 32 + lz) * 4 - 128; wxa = sb + O_WX + q * 128 + hz * 16; ca = sb + O_CNT + 36;
    u32 mg[3];
#pragma unroll
    for (int i = 0; i < 3; ++i) mg[i] = (u32)(max(p ? ng[min(G0 + i, 4)] : ng[i], 1) - 1);
    nseq = __builtin_amdgcn_readfirstlane(mg[0] | mg[1] << 10 | mg[2] << 20); nseqh = 0;
    ngr = G0 + p * (5 - 2 * G0); chk = (1 - p) << 22;
    once = ((0xfcu >> min(tb[1], 31)) & 1) | p;
    asm volatile("" : "+v"(srd), "+v"(I0), "+v"(I1), "+v"(ga), "+v"(la), "+v"(tra), "+v"(ax0), "+v"(ax1), "+v"(ma), "+v"(pda), "+v"(wxa), "+v"(ca), "+v"(gsv));
  };
  if (p) {
    lds_init();
    if (b >= 160 && !binr) hrows(q);
    { u32x2 f = {~tag[0], 0}; poll<PSLEEP>(rF, lane < 32 ? (64 + lane) * 128 : OOB, tag, f); }
    lds_signal(&gcnt[11], lane);
    __builtin_amdgcn_s_setprio(3);
    asm volatile("buffer_load_dwordx4 %0, %4, %5, 0 offen sc1\n\tbuffer_load_dwordx4 %1, %4, %5, 0 offen offset:1024 sc1\n\tbuffer_load_dword %2, %6, %5, 0 offen sc1\n\tbuffer_load_dword %3, %6, %5, 0 offen offset:128 sc1"
                 : "=&v"(W40), "=&v"(W41), "=&v"(wd0), "=&v"(wd1)
                 : "v"(q * 2048 + lane * 16), "s"(rS4), "v"(8192 + q * 256 + r31 * 4) : "memory");
    bf16x8 wa[32], wb[32];
    const __amdgpu_buffer_rsrc_t rV = mk_rsrc(ws + WS_W1F, 393216);
    auto wld = [&](int s) {
      const u32 o = s < 16 ? q * 32768 + s * 1024 : 131072 + q * 65536 + (s - 16) * 1024;
      wa[s] = __builtin_bit_cast(bf16x8, __builtin_amdgcn_raw_buffer_load_b128(rV, lane * 16, o, 16));
      wb[s] = __builtin_bit_cast(bf16x8, __builtin_amdgcn_raw_buffer_load_b128(rV, lane * 16, o + (s < 16 ? 16384 : 32768), 16));
    };
#pragma unroll
    for (int s = 0; s < 16; ++s) { wld(s); SB(); }
    __builtin_amdgcn_s_setprio(0);
    lds_wait<0>(&gcnt[11], 4);
    const int n8 = lane & 7, u8 = (lane >> 3) & 1, g4 = lane >> 4;
    const u32 bch = (g4 & 1) == u8 ? O_H + (32 + n8) * 512 + 16 * ((g4 >> 1) ^ n8) : O_Z;
    const u32 na = (r31 & 16) + 8 * ((r31 >> 2) & 1) + (r31 & 3) + 4 * ((r31 >> 3) & 1);
    f32x16 c0 = {}, c1 = {}; f32x4 d0 = {}, d1 = {};
    MF_BEGIN(c0, c1, d0, d1);
    kloop<2>(smem, O_H + na * 512 + 16 * (hh ^ (na & 15)), bch, [&](int s, const bf16x8& x, const bf16x8& y) {
      MF32V(c0, x, wa[s]); MF16V(d0, y, wa[s]); MF32V(c1, x, wb[s]); MF16V(d1, y, wb[s]);
    });
    MF_END(c0, c1, d0, d1);
    asm volatile("" : "+v"(W40), "+v"(W41), "+v"(wd0), "+v"(wd1));
#pragma unroll
    for (int m = 0; m < 2; ++m) {
      const f32x16& ca = m ? c1 : c0; const f32x4& cb = m ? d1 : d0;
      u32x4 pa, pb;
#pragma unroll
      for (int i = 0; i < 4; ++i) {
        pa[i] = pk2(ca[2 * i] * SCL, ca[2 * i + 1] * SCL);
        pb[i] = pk2(ca[8 + 2 * i] * SCL, ca[9 + 2 * i] * SCL);
      }
      *(u32x4*)(smem + axa(hh, q, 32 * m + r31)) = pa;
      *(u32x4*)(smem + axa(2 + hh, q, 32 * m + r31)) = pb;
      const u32x2 pc = {pk2(cb[0] * SCL, cb[1] * SCL), pk2(cb[2] * SCL, cb[3] * SCL)};
      *(u32x2*)(smem + axa(4, q, 32 * m + (lane & 15) + 16 * hh) + 8 * (g4 & 1)) = pc;
    }
    lds_signal(&gcnt[8], lane);
    if (binr) {
      lds_wait<0>(&gcnt[6], 4);
      __builtin_amdgcn_s_setprio(3);
    }
#pragma unroll
    for (int s = 16; s < 32; ++s) { wld(s); SB(); }
    c0 = f32x16{}; c1 = f32x16{}; d0 = f32x4{}; d1 = f32x4{};
    MF_BEGIN(c0, c1, d0, d1);
    kloop<2>(smem, O_H + r31 * 512 + 16 * (hh ^ (r31 & 15)), bch, [&](int s, const bf16x8& x, const bf16x8& y) {
      MF32V(c0, wa[16 + s], x); MF16V(d0, wa[16 + s], y); MF32V(c1, wb[16 + s], x); MF16V(d1, wb[16 + s], y);
    });
    MF_END(c0, c1, d0, d1);
    {
      const u32 o = (node0 + (lane >> 3)) * 512 + q * 128 + n8 * 16;
#pragma unroll
      for (int j = 0; j < 4; ++j) {
        u32x4 v;
#pragma unroll
        for (int i = 0; i < 4; ++i) v[i] = j < 2 ? pk2(c0[8 * j + 2 * i], c0[8 * j + 2 * i + 1]) : pk2(c1[8 * j - 16 + 2 * i], c1[8 * j - 15 + 2 * i]);
        __builtin_amdgcn_raw_buffer_store_b128(v, rO, o + j * 4096, 0, 0);
      }
      *(u32x4*)(ws + WS_CX + (size_t)b * 4096 + q * 1024 + lane * 16) = u32x4{pk2(d0[0], d0[1]), pk2(d0[2], d0[3]), pk2(d1[0], d1[1]), pk2(d1[2], d1[3])};
    }
    lds_wait<0>(&gcnt[12], 4);
    counts();
    inputs();
  } else {
    lds_init();
    { u32x2 f = {~tag[0], 0}; poll<PSLEEP>(rF, lane < 24 ? (24 * q + lane) * 128 : OOB, tag, f); }
    lds_signal(&gcnt[19], lane);
    lds_wait<0>(&gcnt[19], 4);
    u32x4 cw0, cw1, small; u32x2 f2;
    const u32 fo2 = lane < 40 ? (192 + 40 * q + lane) * 128 : lane < 56 ? (128 + 16 * q + lane - 40) * 128 : OOB;
    {
      const __amdgpu_buffer_rsrc_t rC = mk_rsrc(ws + WS_CELL + (size_t)b * (NCH * 128), NCH * 128);
      const u32 so = tid < 32 ? 9216 + tid * 16 : tid < 52 ? 9728 + node0 * 8 + (tid - 32) * 16 : OOB;
      asm volatile("buffer_load_dwordx4 %0, %8, %9, 0 offen sc1\n\tbuffer_load_dwordx4 %1, %8, %9, 0 offen offset:16 sc1\n\tbuffer_load_dwordx4 %2, %10, %11, 0 offen sc1\n\t"
                   "buffer_load_dwordx4 %4, %12, %11, 0 offen sc1\n\tbuffer_load_dwordx4 %5, %12, %11, 0 offen offset:1024 sc1\n\t"
                   "buffer_load_dword %6, %13, %11, 0 offen sc1\n\tbuffer_load_dword %7, %13, %11, 0 offen offset:128 sc1\n\t"
                   "buffer_load_dwordx2 %3, %14, %15, 0 offen sc1\n\ts_waitcnt vmcnt(5)"
                   : "=&v"(cw0), "=&v"(cw1), "=&v"(small), "=&v"(f2), "=&v"(W40), "=&v"(W41), "=&v"(wd0), "=&v"(wd1)
                   : "v"(tid * 32), "s"(rC), "v"(so), "s"(rS4), "v"(q * 2048 + lane * 16), "v"(8192 + q * 256 + r31 * 4), "v"(fo2), "s"(rF) : "memory");
    }
    const u32 cw[8] = {cw0[0], cw0[1], cw0[2], cw0[3], cw1[0], cw1[1], cw1[2], cw1[3]};
    const u32 ccnt = (u32)__builtin_amdgcn_update_dpp(0, (int)cw[0], 0x00, 0xf, 0xf, false);
    const u32 j1 = (tid & 3) - 1, ebase = (tid >> 2) * CHE;
    u32 rank[8];
#pragma unroll
    for (int k = 0; k < 8; ++k) {
      rank[k] = ~0u;
      unsigned long long sv;
      asm volatile("v_cmp_lt_u32 vcc, %2, %3\n\ts_and_saveexec_b64 %1, vcc\n\tds_add_rtn_u32 %0, %4, %5\n\ts_mov_b64 exec, %1"
                   : "+v"(rank[k]), "=&s"(sv) : "v"(j1 + 4 * k), "v"(ccnt), "v"(sb + O_CNT + 4 * (cw[k] >> 29)), "v"(1u) : "vcc", "scc", "memory");
    }
    if (ccnt > 31) gcnt[7] = 1;
    if (tid < 52) *(u32x4*)(smem + (tid < 32 ? O_WX + tid * 16 : O_POS + (tid - 32) * 16)) = small;
    u32 v0[8], v1[8];
#pragma unroll
    for (int k = 0; k < 8; ++k) { v0[k] = (ebase + (cw[k] & 4095)) | ((cw[k] >> 26) << 18); v1[k] = (cw[k] >> 3) & 0x7ffe00; }
    asm volatile("s_waitcnt lgkmcnt(0)" : "+v"(rank[0]), "+v"(rank[1]), "+v"(rank[2]), "+v"(rank[3]), "+v"(rank[4]), "+v"(rank[5]), "+v"(rank[6]), "+v"(rank[7]),
                 "+v"(v0[0]), "+v"(v0[1]), "+v"(v0[2]), "+v"(v0[3]), "+v"(v0[4]), "+v"(v0[5]), "+v"(v0[6]), "+v"(v0[7]) :: "memory");
    lds_signal(&gcnt[12], lane);
    lds_wait<0>(&gcnt[12], 4);
    counts();
    auto wait2 = [&]() {
      poll<0>(rF, fo2, tag, f2);
      lds_signal(&gcnt[18], lane);
      lds_wait<0>(&gcnt[18], 4);
      if (b == 0 && tid == 0) __builtin_amdgcn_raw_buffer_store_b64(tag, rB, 0, 0, 16);
    };
    asm volatile("s_waitcnt vmcnt(0)" : "+v"(f2), "+v"(W40), "+v"(W41), "+v"(wd0), "+v"(wd1) :: "memory");
    if (slow) { wait2(); lds_signal(&gcnt[6], lane); }
    if (!slow) {
      const u32 tbp = (u32)tb[1] | (u32)tb[2] << 6 | (u32)tb[3] << 12 | (u32)tb[4] << 18 | (u32)tb[5] << 24;
      auto place = [&](int k) {
        const u32 slot = (((tbp >> (6 * (cw[k] >> 29))) & 63) << 5) - 1 - rank[k];
        unsigned long long sv;
        asm volatile("v_cmp_ne_u32 vcc, -1, %1\n\ts_and_saveexec_b64 %0, vcc\n\tds_write_b32 %2, %3\n\tds_write_b32 %4, %5 offset:3136\n\ts_mov_b64 exec, %0"
                     : "=&s"(sv) : "v"(rank[k]), "v"(sb + O_EL + slot * 4), "v"(v0[k]), "v"(sb + (slot >> 5) * TS + (slot & 7) * 16 + ((slot >> 3) & 3) * 4), "v"(v1[k]) : "vcc", "scc", "memory");
      };
#pragma unroll
      for (int k = 0; k < 5; ++k) place(k);
      if (__builtin_amdgcn_ballot_w64(rank[5] != ~0u)) { place(5); place(6); place(7); }
      lds_signal(&gcnt[13], lane);
      lds_wait<0>(&gcnt[13], 4);
      u32 en[4], so[4], ee[7]; u32x2 ps[4];
      const u32x4 s0v = *(const u32x4*)(smem + T_SRC + (lane >> 3) * 16), s1v = *(const u32x4*)(smem + T_SRC + (lane >> 3) * 16 + tb[G0] * TS);
#pragma unroll
      for (int i = 0; i < 4; ++i) {
        const u32 s = min(tid + 256 * i, TCAP * 32 - 1);
        en[i] = *(const u32*)(smem + O_EL + s * 4);
        so[i] = *(const u32*)(smem + T_SRC + (s >> 5) * TS + (s & 7) * 16 + ((s >> 3) & 3) * 4);
        if (i == 3 && tid >= TCAP * 32 - 768) en[i] = ~0u;
      }
#pragma unroll
      for (int i = 0; i < 7; ++i) ee[i] = *(const u32*)(smem + O_EL + (min(q + 4 * i, TCAP - 1) * 32 + r31) * 4);
      const u32 gs1 = (gs0 ^ 64) - 1024, gs2 = gs0 - 2048, gs3 = (gs0 ^ 64) - 3072;
#pragma unroll
      for (int i = 0; i < 3; ++i) asm volatile("buffer_load_dwordx2 %0, %1, %2, 0 offen sc1" : "=v"(ps[i]) : "v"(en[i] == ~0u ? OOB : so[i] >> 6), "s"(rP) : "memory");
      const bool tail = 32 * tb[5] > 768 + 64 * q;
      if (tail) {
        asm volatile("buffer_load_dwordx2 %0, %1, %2, 0 offen sc1\n\ts_waitcnt vmcnt(0)" : "=v"(ps[3]) : "v"(en[3] == ~0u ? OOB : so[3] >> 6), "s"(rP) : "memory");
        put(tid + 768, en[3], ps[3]);
      }
      wait2();
      if (q + 24 < tb[5]) dma16c(sb + T_A1 + (q + 24) * TS, ee[6] == ~0u ? OOB : (ee[6] & 0x3ffff) * 32 + 16 * hh, rE);
      asm volatile("s_mov_b32 m0, %0" :: "s"(gm0));
      gd4(s0v, gs0, gs1, gs2, gs3, rB, gso);
#pragma unroll
      for (int i = 0; i < 6; ++i) {
        dma16c(sb + T_A1 + (q + 4 * i) * TS, ee[i] == ~0u ? OOB : (ee[i] & 0x3ffff) * 32 + 16 * hh, rE);
        if (i == 1) {
          asm volatile("s_mov_b32 m0, %0" :: "s"(gm0 + 16384));
          gd4(s1v, gs0, gs1, gs2, gs3, rB, gso);
        }
      }
      lds_signal(&gcnt[6], lane);
      asm volatile("s_waitcnt vmcnt(14)" : "+v"(ps[0]), "+v"(ps[1]), "+v"(ps[2]) :: "memory");
#pragma unroll
      for (int i = 0; i < 3; ++i) put(tid + 256 * i, en[i], ps[i]);
    }
    inputs();
    if (tb[1] > 1 && tb[1] < 8) asm volatile("s_waitcnt vmcnt(8)" ::: "memory");
    else { asm volatile("s_waitcnt vmcnt(0)" ::: "memory"); lds_signal(&gcnt[15], lane); }
    asm volatile("" : "+v"(W40), "+v"(W41), "+v"(wd0), "+v"(wd1));
    lds_signal(&gcnt[8], lane);
  }
  if (!slow) {
    {
      lds_wait<0>(&gcnt[8 + 7 * p], 8 - 4 * p);
      if (p) __builtin_amdgcn_s_setprio(1); else __builtin_amdgcn_s_setprio(2);
      asm volatile("s_mov_b32 m0, %0" :: "s"(gm0));
      asm volatile(EDGE_ASM
                   : "+{v240}"(la), "+{v241}"(tra), "+{v242}"(srd), "+{v243}"(ga), "+{v247}"(gsv), "+{v251}"(ax0), "+{v252}"(ax1), "+{v253}"(ma),
                     "+{v254}"(pda), "+{v28}"(wxa), "+{v30}"(ca), [ngr] "+&s"(ngr), [tmp] "=&s"(tmp), [chk] "+&s"(chk)
                   : "{v26}"(wd0), "{v27}"(wd1), "{v[180:183]}"(W40), "{v[184:187]}"(W41), "{v[204:207]}"(I0), "{v[208:211]}"(I1), [rb] "s"(rB),
                     [gso] "s"(gso), [nseq] "s"(nseq), [nseqh] "s"(nseqh), [sig] "s"(p), [fin] "s"(16 * p), [once] "s"(once)
                   : EDGE_CLOB);
    }
  } else {
    int st = threadIdx.x;
    asm volatile("" : "+v"(st));
    const int sl = st & 63;
    float* Af = (float*)smem;
    float* Mf = (float*)(smem + 40960);
    float* pf = (float*)(smem + 81920);
    u32* lst = (u32*)(smem + O_G);
    BAR_ALL();
    for (int i = st; i < NPB * 256; i += 512) {
      const int n = i >> 8, f = i & 255;
      float a = 0.f;
      for (int k = 0; k < 256; ++k) a += bf2f(h[min(node0 + n, N - 1) * 256 + k]) * bf2f(we[k * 256 + f]);
      Af[i] = a * SCL; Mf[i] = 0.f;
    }
    if (st < 160) pf[st] = 0.f;
    for (int e0 = 0; e0 < E; e0 += 512) {
      if (st == 0) gcnt[6] = 0;
      __syncthreads();
      const int e = e0 + st;
      if (e < E) { const u32 dl = (u32)(dstg[e] - node0); if (dl < (u32)(nend - node0)) lst[atomicAdd(&gcnt[6], 1u)] = e; }
      __syncthreads();
      const int cnt = gcnt[6];
      for (int i = w; i < cnt; i += 8) {
        const int ee = lst[i], s = srcg[ee], dl = dstg[ee] - node0;
        const u32x2 pd = *(const u32x2*)(smem + O_POS + dl * 8), psv = __builtin_bit_cast(u32x2, __builtin_amdgcn_raw_buffer_load_b64(rP, s * 8, 0, 16));
        const float fx = bf2f(f2bf(bf2f(pd[0] & 0xffff) - bf2f(psv[0] & 0xffff))), fy = bf2f(f2bf(bf2f(pd[0] >> 16) - bf2f(psv[0] >> 16)));
        const float fz = bf2f(f2bf(bf2f(pd[1] & 0xffff) - bf2f(psv[1] & 0xffff)));
        const float d2 = bf2f(f2bf(bf2f(f2bf(fx * fx)) + bf2f(f2bf(fy * fy)) + bf2f(f2bf(fz * fz))));
        float cs = 0.f;
        for (int c = 0; c < 4; ++c) {
          const int f = sl + 64 * c;
          float y = d2 * bf2f(we[512 * 256 + f]);
          for (int k = 0; k < 16; ++k) y += bf2f(ea[ee * 16 + k]) * bf2f(we[(513 + k) * 256 + f]);
          y = y * SCL + Af[dl * 256 + f] + bf2f(__builtin_amdgcn_raw_buffer_load_b16(rB, 4096 + s * 512 + f * 2, 0, 16));
          const float m = bf2f(f2bf(y * __builtin_amdgcn_rcpf(1.f + __builtin_amdgcn_exp2f(y))));
          atomicAdd(&Mf[dl * 256 + f], m);
          cs += m * bf2f(wxg[f]);
        }
        for (int o = 32; o; o >>= 1) cs += __shfl_xor(cs, o);
        if (sl < 3) atomicAdd(&pf[dl * 4 + sl], cs * ISCL * (sl == 0 ? fx : sl == 1 ? fy : fz));
      }
      __syncthreads();
    }
    for (int i = st; i < NPB * 256; i += 512) {
      const int n = i >> 8, f = i & 255;
      *(u16*)(smem + O_M + n * 512 + ((f >> 7) << 8) + 16 * (((f >> 3) & 15) ^ (n & 15)) + 2 * (f & 7)) = (u16)f2bf(Mf[i]);
    }
    float pv = 0.f;
    if (st < 160 && (st & 31) >= 8) pv = pf[((st >> 5) * 8 + (st & 7)) * 4 + ((st >> 3) & 3) - 1];
    __syncthreads();
    if (st < 160) { pdp[st] = pv; pdp[160 + st] = 0.f; pdp[320 + st] = 0.f; pdp[480 + st] = 0.f; }
    if (st == 0) { gcnt[9] = 8; gcnt[10] = 4; gcnt[15] = 8; gcnt[13] = 8; }
    __syncthreads();
  }
  int ln = threadIdx.x;
  asm volatile("" : "+v"(ln));
  if (!p) {
    __builtin_amdgcn_s_setprio(2);
    const int l63 = ln & 63, l31 = ln & 31, lh = (ln >> 5) & 1, m8 = ln & 7, v8 = (ln >> 3) & 1, h4 = (ln >> 4) & 3;
    const __amdgpu_buffer_rsrc_t rV = mk_rsrc(ws + WS_WHF + q * 65536 + 16384, 49152);
    const u32 oo = (node0 + (l63 >> 3)) * 512 + q * 128 + m8 * 16;
    lds_wait<1>(&gcnt[15], 8);
    u32x4 cc[5];
#pragma unroll
    for (int j = 0; j < 4; ++j) cc[j] = __builtin_bit_cast(u32x4, __builtin_amdgcn_raw_buffer_load_b128(rO, oo + 4096 * j, 0, 0));
    cc[4] = *(const u32x4*)(ws + WS_CX + (size_t)b * 4096 + q * 1024 + l63 * 16);
    bf16x8 wa[16], wb[16];
#pragma unroll
    for (int s = 0; s < 16; ++s) {
      wa[s] = __builtin_bit_cast(bf16x8, __builtin_amdgcn_raw_buffer_load_b128(rV, l63 * 16, s * 1024, 16));
      wb[s] = __builtin_bit_cast(bf16x8, __builtin_amdgcn_raw_buffer_load_b128(rV, l63 * 16, 32768 + s * 1024, 16));
      SB();
      if (s >= WD - 1 && s < 15) asm volatile("s_waitcnt vmcnt(%0)" :: "i"(2 * WD - 2) : "memory");
    }
    f32x16 c0, c1; f32x4 d0, d1;
#pragma unroll
    for (int i = 0; i < 8; ++i) {
      c0[2 * i] = bf2f(cc[i >> 2][i & 3] & 0xffff); c0[2 * i + 1] = __uint_as_float(cc[i >> 2][i & 3] & 0xffff0000u);
      c1[2 * i] = bf2f(cc[2 + (i >> 2)][i & 3] & 0xffff); c1[2 * i + 1] = __uint_as_float(cc[2 + (i >> 2)][i & 3] & 0xffff0000u);
    }
    d0 = f32x4{bf2f(cc[4][0] & 0xffff), __uint_as_float(cc[4][0] & 0xffff0000u), bf2f(cc[4][1] & 0xffff), __uint_as_float(cc[4][1] & 0xffff0000u)};
    d1 = f32x4{bf2f(cc[4][2] & 0xffff), __uint_as_float(cc[4][2] & 0xffff0000u), bf2f(cc[4][3] & 0xffff), __uint_as_float(cc[4][3] & 0xffff0000u)};
    asm volatile("" : "+v"(c0), "+v"(c1), "+v"(d0), "+v"(d1));
    const u32 ba = l31 * 512 + 16 * (lh ^ (l31 & 15));
    __builtin_amdgcn_s_setprio(0);
    lds_wait<2>(&gcnt[9], 8);
    kloop<1>(smem, O_M + ba, 0, [&](int s, const bf16x8& x, const bf16x8&) { c0 = MFMA32(wa[s], x, c0); c1 = MFMA32(wb[s], x, c1); });
    auto silu = [](float z) { return z * __builtin_amdgcn_rcpf(1.f + __builtin_amdgcn_exp2f(z)) * ISCL; };
    char* tb = smem + O_G + w * 4096;
    {
      const u32 ch = 8 * q + 4 * lh;
      const u32 ha = O_H + l31 * 512 + (ch >> 4) * 256 + 16 * ((ch & 15) ^ (l31 & 15)), wa2 = l31 * 128 + 16 * ((4 * lh) ^ (l31 & 7));
#pragma unroll
      for (int j = 0; j < 4; ++j) {
        const u32x4 hv = *(const u32x4*)(smem + (ha ^ (16 * j)));
        u32x4 v;
#pragma unroll
        for (int i = 0; i < 4; ++i) {
          const float z0 = j < 2 ? c0[8 * j + 2 * i] : c1[8 * j - 16 + 2 * i], z1 = j < 2 ? c0[8 * j + 2 * i + 1] : c1[8 * j - 15 + 2 * i];
          v[i] = pk2(bf2f(hv[i] & 0xffff) + silu(z0), bf2f(hv[i] >> 16) + silu(z1));
        }
        *(u32x4*)(tb + (wa2 ^ (16 * j))) = v;
      }
      const u32 ra = (l63 >> 3) * 128 + 16 * (m8 ^ (l63 >> 3));
#pragma unroll
      for (int j = 0; j < 4; ++j) __builtin_amdgcn_raw_buffer_store_b128(*(const u32x4*)(tb + ra + 1024 * j), rO, oo + 4096 * j, 0, 16);
    }
    const u32 fl = 32 * (h4 & 1) + 4 * (2 * v8 + (h4 >> 1)), ch = 8 * q + (fl >> 3), hr = O_H + (32 + m8) * 512 + (ch >> 4) * 256 + 2 * (fl & 7);
    u32x2 hv0 = *(const u32x2*)(smem + hr + 16 * ((ch & 15) ^ m8)), hv1 = *(const u32x2*)(smem + hr + 16 * (((ch + 2) & 15) ^ m8));
    u32 ya[8];
#pragma unroll
    for (int s = 0; s < 8; ++s) ya[s] = ((h4 & 1) == v8 ? O_M + (32 + m8) * 512 + 16 * ((h4 >> 1) ^ m8) : O_Z) ^ (32 * s);
    asm volatile("" : "+v"(hv0), "+v"(hv1), "+v"(ya[0]), "+v"(ya[1]), "+v"(ya[2]), "+v"(ya[3]), "+v"(ya[4]), "+v"(ya[5]), "+v"(ya[6]), "+v"(ya[7]));
    __builtin_amdgcn_s_setprio(3);
    lds_wait<0>(&gcnt[10], 4);
    {
      bf16x8 y[16];
#pragma unroll
      for (int s = 0; s < 16; ++s) y[s] = ld128(smem, ya[s & 7] + 256 * (s >> 3));
      SB();
#pragma unroll
      for (int s = 0; s < 16; ++s) { d0 = MFMA16(wa[s], y[s], d0); d1 = MFMA16(wb[s], y[s], d1); }
    }
    *(u32x2*)(tb + m8 * 128 + 2 * fl) = u32x2{pk2(bf2f(hv0[0] & 0xffff) + silu(d0[0]), bf2f(hv0[0] >> 16) + silu(d0[1])), pk2(bf2f(hv0[1] & 0xffff) + silu(d0[2]), bf2f(hv0[1] >> 16) + silu(d0[3]))};
    *(u32x2*)(tb + m8 * 128 + 2 * fl + 32) = u32x2{pk2(bf2f(hv1[0] & 0xffff) + silu(d1[0]), bf2f(hv1[0] >> 16) + silu(d1[1])), pk2(bf2f(hv1[1] & 0xffff) + silu(d1[2]), bf2f(hv1[1] >> 16) + silu(d1[3]))};
    __builtin_amdgcn_raw_buffer_store_b128(*(const u32x4*)(tb + l63 * 16), rO, oo + 16384, 0, 16);
  } else if (w < 6) {
    lds_wait<1>(&gcnt[9], 8);
    lds_wait<1>(&gcnt[13], 8);
    const int t = ln - 256;
    if (t < 3 * (nend - node0)) {
      const int n = t / 3, c = t - 3 * n, i = (n >> 3) * 32 + 8 * (c + 1) + (n & 7);
      const float d = (pdp[i] + pdp[160 + i]) + (pdp[320 + i] + pdp[480 + i]);
      const u32 pv = ((const u16*)(smem + O_POS))[n * 4 + c];
      out[(size_t)N * 256 + node0 * 3 + t] = (u16)f2bf(bf2f(pv) + d);
    }
  }
  asm volatile("s_waitcnt vmcnt(0)" ::: "memory");
}

extern "C" void kernel_launch(void* const* d_in, const int* in_sizes, int n_in, void* d_out, int out_size, void* d_ws, size_t ws_size,
                              hipStream_t stream) {
  egnn<<<NB, 512, 0, stream>>>((const u16*)d_in[0], (const u16*)d_in[1], (const u16*)d_in[7], (const u16*)d_in[2], (const int*)d_in[3], (const int*)d_in[4],
                               (const u16*)d_in[5], (const u16*)d_in[6], (char*)d_ws, (u16*)d_out);
}
```
